```python
import torch

def _softmax(x, axis=-1):
    return torch.softmax(x, dim=axis)

def _gelu(x):
    return torch.nn.functional.gelu(x, approximate='tanh')

def _silu(x):
    return torch.nn.functional.silu(x)

def _sigmoid(x):
    return torch.sigmoid(x)

def _layernorm(x, axis=-1, eps=1e-05):
    m = x.mean(dim=axis, keepdim=True)
    v = x.var(dim=axis, keepdim=True, unbiased=False)
    return (x - m) / torch.sqrt(v + eps)

def _scatter_add(tgt, idx, src, axis=0):
    return tgt.clone().index_add_(axis, idx, src)

def _segment_sum(src, seg, n):
    out = torch.zeros((n,) + src.shape[1:], dtype=src.dtype)
    return out.index_add_(0, seg, src)

def _pad(x, pw):
    flat = tuple((v for pair in reversed(pw) for v in pair))
    return torch.nn.functional.pad(x, flat)
kernel_name = 'adaLN_cond__spectral_gfnet__glu_gated_swiglu__prompt'

def setup_inputs():

    _w = lambda a: (lambda t: t.to(torch.bfloat16) if t.is_floating_point() else t)(torch.as_tensor(a))

    def _A_setup():
        B, S, D = (1, 2048, 512)
        Df = D // 2 + 1
        return {'x': _w((lambda *s: torch.randn(*s, dtype=torch.bfloat16) * 0.05)(B, S, D)), 'w_r': _w((lambda *s: torch.randn(*s, dtype=torch.bfloat16) * 1.6)(S, Df) * 0.02), 'w_i': _w((lambda *s: torch.randn(*s, dtype=torch.bfloat16) * 3.2)(S, Df) * 0.02)}
    _A_in = _A_setup()

    def _B_setup():
        B, S, D, F = (1, 2048, 512, 1792)
        return {'x': _w((lambda *s: torch.randn(*s, dtype=torch.bfloat16) * 0.05)(B, S, D)), 'w1': _w((lambda *s: torch.randn(*s, dtype=torch.bfloat16) * 0.4)(D, F) * (2.0 / D) ** 0.5), 'w3': _w((lambda *s: torch.randn(*s, dtype=torch.bfloat16) * 0.4)(D, F) * (2.0 / D) ** 0.5), 'w2': _w((lambda *s: torch.randn(*s, dtype=torch.bfloat16) * 0.4)(F, D) * (2.0 / F) ** 0.5)}
    _B_in = _B_setup()
    return {'A_x': _A_in['x'], 'A_w_r': _A_in['w_r'], 'A_w_i': _A_in['w_i'], 'B_x': _B_in['x'], 'B_w1': _B_in['w1'], 'B_w3': _B_in['w3'], 'B_w2': _B_in['w2'], 'c': _w((lambda *s: torch.randn(*s, dtype=torch.bfloat16) * 0.1)(1, 512) * 8.0), 'w_mod': _w((lambda *s: torch.randn(*s, dtype=torch.bfloat16) * 0.1)(512, 6 * 512) * 512 ** (-0.5) * 8.0)}

def reference(A_x, A_w_r, A_w_i, B_x, B_w1, B_w3, B_w2, c, w_mod):

    def _A_ref(x, w_r, w_i):
        B, S, D = x.shape
        X = (lambda x, axes=(-2, -1): torch.fft.rfft2(x.float(), dim=axes))(x, axes=(-2, -1))
        W = w_r + 1j * w_i
        X = X * W
        y = (lambda x, s, axes=(-2, -1): torch.fft.irfft2(x, s=s, dim=axes).to(torch.bfloat16))(X, (S, D), axes=(-2, -1))
        return y

    def _B_ref(x, w1, w3, w2):
        gate = torch.einsum('bsd,df->bsf', x, w1)
        up = torch.einsum('bsd,df->bsf', x, w3)
        h = _silu(gate) * up
        out = torch.einsum('bsf,fd->bsd', h, w2)
        return out
    x = A_x
    mod = (_silu(c) @ w_mod).reshape(c.shape[0], 6, 1, -1)
    sa, ca, ga, sb, cb, gb = (mod[:, 0].clone(), mod[:, 1].clone(), mod[:, 2].clone(), mod[:, 3].clone(), mod[:, 4].clone(), mod[:, 5].clone())
    h = _layernorm(x, axis=-1) * (1 + ca) + sa
    x = x + ga * _A_ref(h, A_w_r, A_w_i)
    h = _layernorm(x, axis=-1) * (1 + cb) + sb
    x = x + gb * _B_ref(h, B_w1, B_w3, B_w2)
    return x
```

```cpp
#include <hip/hip_runtime.h>
#include <hip/hip_bf16.h>

typedef unsigned u32;
typedef unsigned short u16;
typedef u32 v4u __attribute__((ext_vector_type(4)));
typedef u32 v2u __attribute__((ext_vector_type(2)));
typedef float f2 __attribute__((ext_vector_type(2)));
typedef float f4 __attribute__((ext_vector_type(4)));
typedef float f16v __attribute__((ext_vector_type(16)));
typedef short s4 __attribute__((ext_vector_type(4)));
typedef __bf16 b8 __attribute__((ext_vector_type(8)));
typedef __bf16 b2 __attribute__((ext_vector_type(2)));
typedef __amdgpu_buffer_rsrc_t rsrc_t;
#define LAS __attribute__((address_space(3)))
#define DEV __device__ __forceinline__

constexpr u32 WS_EPOCH = 0;
constexpr u32 WS_G0 = 4096;
constexpr u32 WS_G1 = 8192;
constexpr u32 XSTR = 8320;
constexpr u32 WS_X1 = 65536;
constexpr u32 WS_WT = WS_X1 + 256 * XSTR;
constexpr u32 WS_X2 = WS_WT + 256 * XSTR;
constexpr u32 WS_H2 = WS_X2 + 256 * XSTR;
constexpr u32 WS_XR = WS_H2 + 2048 * 1024;
constexpr u32 WS_HH = WS_XR + 2048 * 1024;
constexpr u32 WS_END = WS_HH + 2048 * 1792 * 2;
constexpr u32 WS_DBG = WS_END;

constexpr u32 L_W1 = 0, L_W3 = 57344 + 128;
constexpr u32 L_RING = 114816;
constexpr u32 SLOT = 16896;
constexpr u32 L_XS = L_RING;
constexpr u32 L_T0 = L_RING + 8192;
constexpr u32 L_T1 = L_T0 + 8320;
constexpr u32 L_E1 = L_XS;
constexpr u32 L_E2 = L_T1 + 8320;
constexpr u32 L_MOD0 = L_RING + 2 * SLOT;
constexpr u32 L_MOD1 = L_MOD0 + 2048;
constexpr u32 L_B7 = L_MOD1 + 4096;
constexpr u32 L_TOTAL = L_B7 + 8192;
static_assert(L_E2 + 8 * 1088 <= L_MOD0, "lds");
static_assert(L_TOTAL <= 163840, "lds");

constexpr u32 SENT = 0xFFFFFFFFu;
constexpr u32 SPIN_CAP = 1u << 16;
constexpr int SC1 = 16;

#define D_E0 8
#define D_E1 0
#define D_E2 0
#define D_E3 0

extern __shared__ __attribute__((aligned(16))) unsigned char smem[];

DEV u32 pk(float lo, float hi) { f2 v = {lo, hi}; return __builtin_bit_cast(u32, __builtin_convertvector(v, b2)); }
DEV float lo16(u32 x) { return __builtin_bit_cast(float, x << 16); }
DEV float hi16(u32 x) { return __builtin_bit_cast(float, x & 0xFFFF0000u); }
DEV float bfr(float x) { return lo16(pk(x, x)); }
DEV float cosr(float rev) { return __builtin_amdgcn_cosf(rev); }
DEV float sinr(float rev) { return __builtin_amdgcn_sinf(rev); }
DEV f4 mfma16(v4u a, v4u b, f4 c) { return __builtin_amdgcn_mfma_f32_16x16x32_bf16(__builtin_bit_cast(b8, a), __builtin_bit_cast(b8, b), c, 0, 0, 0); }
DEV f4 mfma16k(v2u a, v2u b, f4 c) { return __builtin_amdgcn_mfma_f32_16x16x16bf16_1k(__builtin_bit_cast(s4, a), __builtin_bit_cast(s4, b), c, 0, 0, 0); }
DEV f16v mfma32(v4u a, v4u b, f16v c) { return __builtin_amdgcn_mfma_f32_32x32x16_bf16(__builtin_bit_cast(b8, a), __builtin_bit_cast(b8, b), c, 0, 0, 0); }
DEV v2u ldtr(u32 off) { return __builtin_bit_cast(v2u, __builtin_amdgcn_ds_read_tr16_b64_v4i16((LAS s4*)(smem + off))); }
DEV v4u ldtr2(u32 off0, u32 off1) { v2u a = ldtr(off0), b = ldtr(off1); return v4u{a.x, a.y, b.x, b.y}; }
template <class T> DEV T& lds(u32 off) { return *(T*)(smem + off); }
DEV rsrc_t mkrs(const void* p, u32 bytes) { return __builtin_amdgcn_make_buffer_rsrc((void*)p, 0, bytes, 0x00020000); }
DEV v4u mkrs4(const void* p, u32 bytes) { unsigned long long a = (unsigned long long)p; return v4u{(u32)a, (u32)(a >> 32) & 0xFFFFu, bytes, 0x00020000u}; }
DEV v4u ld128(rsrc_t r, u32 off) { return __builtin_amdgcn_raw_buffer_load_b128(r, off, 0, 0); }
DEV v2u ld64(rsrc_t r, u32 off) { return __builtin_amdgcn_raw_buffer_load_b64(r, off, 0, 0); }
DEV u32 ld16(rsrc_t r, u32 off) { return (u32)__builtin_amdgcn_raw_buffer_load_b16(r, off, 0, 0); }
DEV v4u ld128c(rsrc_t r, u32 off, u32 soff = 0) { return __builtin_amdgcn_raw_buffer_load_b128(r, off, soff, SC1); }
DEV v2u ld64c(rsrc_t r, u32 off) { return __builtin_amdgcn_raw_buffer_load_b64(r, off, 0, SC1); }
DEV void st128c(v4u v, rsrc_t r, u32 off, u32 soff = 0) { __builtin_amdgcn_raw_buffer_store_b128(v, r, off, soff, SC1); }
DEV void dma16(v4u rs, u32 voff, u32 soff, u32 ldsa) {
    asm volatile("s_mov_b32 m0, %3\n\ts_nop 0\n\tbuffer_load_dwordx4 %0, %1, %2 offen lds" ::"v"(voff), "s"(rs), "s"(soff), "s"(ldsa) : "memory");
}
#define VM0() asm volatile("s_waitcnt vmcnt(0)" ::: "memory")
#define BAR() do { asm volatile("s_waitcnt lgkmcnt(0)" ::: "memory"); __builtin_amdgcn_s_barrier(); asm volatile("" ::: "memory"); } while (0)
#define SCHED() __builtin_amdgcn_sched_barrier(0)
#define PIN(x) asm volatile("" : "+v"(x))
#define RETRY(BAD, LOAD) do { if (__builtin_expect(anylane(BAD), 0)) { u32 sp_ = 0; \
        _Pragma("nounroll") do { asm volatile("" ::: "memory"); LOAD; } while (anylane(BAD) && ++sp_ < SPIN_CAP); } } while (0)
template <int N> DEV void nap() { if (N > 0) __builtin_amdgcn_s_sleep(N); }
DEV u32 umax3(u32 a, u32 b, u32 c) { return max(max(a, b), c); }
DEV u32 vmax(v4u v, u32 m) { return umax3(umax3(m, v.x, v.y), v.z, v.w); }
template <int N> DEV u32 vmaxn(const v4u (&v)[N]) { u32 m = 0;
#pragma unroll
    for (int i = 0; i < N; ++i) m = vmax(v[i], m);
    return m; }
DEV int fresh_lane() { int x; asm volatile("v_mbcnt_lo_u32_b32 %0, -1, 0\n\tv_mbcnt_hi_u32_b32 %0, -1, %0" : "=v"(x)); return x; }
DEV bool anylane(bool b) { return __builtin_amdgcn_ballot_w64(b) != 0ull; }

template <int CTRL, int RM> DEV float dppadd(float v) {
    return v + __builtin_bit_cast(float, __builtin_amdgcn_update_dpp(0, __builtin_bit_cast(int, v), CTRL, RM, 0xF, true));
}
DEV float wave_sum(float v) {
    v = dppadd<0xB1, 0xF>(v); v = dppadd<0x4E, 0xF>(v); v = dppadd<0x141, 0xF>(v); v = dppadd<0x140, 0xF>(v);
    v = dppadd<0x142, 0xF>(v); v = dppadd<0x143, 0xF>(v);
    return __builtin_bit_cast(float, __builtin_amdgcn_readlane(__builtin_bit_cast(int, v), 63));
}
DEV float silu(float x) { return x * __builtin_amdgcn_rcpf(1.0f + __builtin_amdgcn_exp2f(-1.44269504089f * x)); }

DEV void fwd_pair(float rev0, float drev, v4u& R, v4u& I) {
#pragma unroll
    for (int r = 0; r < 4; ++r) { const float a = rev0 + r * drev, c = cosr(a), s = sinr(a); R[r] = pk(c, s); I[r] = pk(-s, c); }
}
DEV v4u twid_pack(f4 re, f4 im, const f4& c, const f4& s) {
    v4u o;
#pragma unroll
    for (int r = 0; r < 4; ++r) o[r] = pk(re[r] * c[r] + im[r] * s[r], im[r] * c[r] - re[r] * s[r]);
    return o;
}
DEV int row_of(int L, int rho) { return 128 * (4 * ((L >> 3) & 3) + (rho & 3)) + 8 * (2 * (L & 7) + (rho >> 2)) + (L >> 5); }

#define STAMP(i)

__global__ void __launch_bounds__(512, 2)
fused_kernel(const u16* __restrict__ px, const u16* __restrict__ pwr, const u16* __restrict__ pwi, const u16* __restrict__ pw1,
             const u16* __restrict__ pw3, const u16* __restrict__ pw2, const u16* __restrict__ pc, const u16* __restrict__ pwm,
             u16* __restrict__ pout, unsigned char* __restrict__ ws) {
    const int t = threadIdx.x, l = t & 63, li = l & 15, g = l >> 4;
    const int w = __builtin_amdgcn_readfirstlane(t >> 6);
    const int bid = blockIdx.x, L = (bid & 7) * 32 + (bid >> 3);
    const int P = (L >> 6) * 4 + ((L >> 3) & 3), mem = ((L >> 5) & 1) * 8 + (L & 7);
    const rsrc_t rws = mkrs(ws, WS_END);
    const f4 z4 = {0.f, 0.f, 0.f, 0.f};
    const v4u sent4 = {SENT, SENT, SENT, SENT};
    STAMP(0);

    const rsrc_t rwm = mkrs(pwm, 512 * 3072 * 2);
    v2u e0[8]; v4u cv = {0, 0, 0, 0};
    if (w == 7) {
#pragma unroll
        for (int i = 0; i < 8; ++i) e0[i] = ld64(rwm, (8 * l + i) * 6144 + 8 * L);
    }
    if (w >= 6) cv = ld128(mkrs(pc, 1024), 16 * l);
    const u32 tag = __builtin_amdgcn_raw_buffer_load_b32(rws, WS_EPOCH, 0, SC1) + 1u;
    u32 cpf;
    {
        unsigned long long pcv; u32 len;
        asm volatile("s_getpc_b64 %0\n.Lpcref%=:\n\ts_mov_b32 %1, .Lcode_tail-.Lpcref%=" : "=s"(pcv), "=s"(len));
        cpf = __builtin_amdgcn_raw_buffer_load_b32(mkrs((const void*)pcv, len), t * 128, 0, 0);
    }
    const int srow = row_of(L, w);
    const v4u xv = ld128(mkrs(px, 2048 * 1024), srow * 1024 + 16 * l);
    u32 wre[4], wim[4], wx[6];
    {
        const int tt = 2 * L + (w >> 2), j = w & 3;
        const int pos = 32 * (tt >> 6) + 2 * (tt & 15) + (j >> 1), kb = 2 * ((tt >> 4) & 3) + (j & 1);
        const int k = ((pos >> 2) & 15) + 16 * (4 * (pos >> 6) + (pos & 3)) + 256 * kb, km = (2048 - k) & 2047;
        const rsrc_t rr = mkrs(pwr, 2048 * 257 * 2), ri = mkrs(pwi, 2048 * 257 * 2);
#pragma unroll
        for (int r = 0; r < 4; ++r) {
            const int kk = 4 * g + r;
            const int k2 = li ? (kk < 8 ? li + 32 * kk : 512 - li - 32 * kk) : (kk < 8 ? 32 * kk : 32 * kk - 240);
            wre[r] = ld16(rr, (k * 257 + k2) * 2); wim[r] = ld16(ri, (k * 257 + k2) * 2);
        }
        wx[0] = ld16(rr, (k * 257 + 256) * 2); wx[1] = ld16(ri, (k * 257 + 256) * 2);
        wx[2] = ld16(rr, km * 514); wx[3] = ld16(ri, km * 514);
        wx[4] = ld16(rr, (km * 257 + 256) * 2); wx[5] = ld16(ri, (km * 257 + 256) * 2);
    }
    v4u e1[8];
    if (w == 6) {
#pragma unroll
        for (int i = 0; i < 8; ++i) e1[i] = ld128(rwm, (8 * l + i) * 6144 + 2048 + 16 * L);
    }
    const int ft = 7 * mem + w;
    const u32 hh_w = WS_HH + (P * 8 + ((l >> 4) & 1)) * 57344 + (ft >> 1) * 1024 + ((ft & 1) * 2 + (l >> 5)) * 256 + li * 16;
    const u32 h2_w = (P * 128 + mem * 8 + w) * 1024 + (4 * li + g) * 16;
    if (w == 7) {
#pragma unroll
        for (int i = 0; i < 8; ++i) st128c(sent4, rws, WS_X1 + L * XSTR + i * 1024 + l * 16);
    } else {
#pragma unroll
        for (int q = 0; q < 4; ++q) st128c(sent4, rws, hh_w + q * 114688);
    }
    st128c(sent4, rws, WS_X2 + L * XSTR + t * 16);
    st128c(sent4, rws, WS_H2 + h2_w);
    SCHED();

    const float fl = (float)li, fg4 = (float)(4 * g), fw = (float)w;
    v4u aBR, aBI, aAI, aSR, aSI, FR, FI, GR, GI, HR, HI;
    f4 tc, ts, bc, bs, bc2, bs2;
    v2u A8;
    {
#pragma unroll
        for (int h = 0; h < 2; ++h)
#pragma unroll
            for (int jj = 0; jj < 2; ++jj) {
                const float n0 = fg4 + 16 * h + 2 * jj, a0 = n0 * fl * (1.f / 32), a1 = (n0 + 1.f) * fl * (1.f / 32);
                aBR[2 * h + jj] = pk(cosr(a0), cosr(a1));
                aBI[2 * h + jj] = li ? pk(-sinr(a0), -sinr(a1)) : pk(1.f, -1.f);
            }
        fwd_pair(fg4 * fl * (1.f / 16), fl * (1.f / 16), FR, FI);
        const float sm = li < 8 ? fl * (1.f / 16) : (2.f * fl - 15.f) * (1.f / 32);
#pragma unroll
        for (int r = 0; r < 4; ++r) {
            aAI[r] = li < 8 ? FI[r] : FI[r] ^ 0x80008000u;
            const float a = (fg4 + r) * sm, c = cosr(a), s = li ? -sinr(a) : ((r & 1) ? -1.f : 1.f);
            aSR[r] = li < 8 ? pk(c, 0.f) : pk(0.f, c);
            aSI[r] = li < 8 ? pk(s, 0.f) : pk(0.f, s);
            const float a2 = (fg4 + r) * fl * (1.f / 512);
            tc[r] = cosr(a2); ts[r] = sinr(a2);
            const float a3 = (8.f * (fg4 + r) + fw) * fl * (1.f / 2048);
            bc[r] = cosr(a3); bs[r] = sinr(a3);
            const float a4 = (8.f * fl + fw) * (fg4 + r) * (1.f / 2048);
            bc2[r] = cosr(a4); bs2[r] = sinr(a4);
        }
        fwd_pair((8.f * fg4 + fw) * fl * (1.f / 128), fl * (1.f / 16), GR, GI);
        fwd_pair((8.f * fl + fw) * fg4 * (1.f / 128), (8.f * fl + fw) * (1.f / 128), HR, HI);
#pragma unroll
        for (int jb = 0; jb < 2; ++jb) {
            const float a = (float)((2 * g + jb) * (li >> 1)) * 0.125f, c = cosr(a), s = sinr(a);
            A8[jb] = (li & 1) ? pk(-s, c) : pk(c, s);
        }
        PIN(aBR); PIN(aBI); PIN(aAI); PIN(aSR); PIN(aSI); PIN(FR); PIN(FI); PIN(GR); PIN(GI); PIN(HR); PIN(HI);
        PIN(tc); PIN(ts); PIN(bc); PIN(bs); PIN(bc2); PIN(bs2); PIN(A8);
    }
    SCHED();
    STAMP(1);

    v4u g1a = {0, 0, 0, 0};
    if (w >= 6) {
        float sc[8];
#pragma unroll
        for (int i = 0; i < 4; ++i) { sc[2 * i] = bfr(silu(lo16(cv[i]))); sc[2 * i + 1] = bfr(silu(hi16(cv[i]))); }
        if (w == 7) {
            f4 a = z4;
#pragma unroll
            for (int i = 0; i < 8; ++i) { a[0] += sc[i] * lo16(e0[i].x); a[1] += sc[i] * hi16(e0[i].x); a[2] += sc[i] * lo16(e0[i].y); a[3] += sc[i] * hi16(e0[i].y); }
            const float one = L >= 128 ? 1.f : 0.f;
#pragma unroll
            for (int i = 0; i < 4; ++i) a[i] = bfr(wave_sum(a[i])) + one;
            const v4u gr = {pk(a[0], a[1]), tag, pk(a[2], a[3]), tag};
            VM0();
            if (l == 0) st128c(gr, rws, WS_G0 + 16 * L);
        } else {
            float a[8] = {0.f, 0.f, 0.f, 0.f, 0.f, 0.f, 0.f, 0.f};
#pragma unroll
            for (int i = 0; i < 8; ++i)
#pragma unroll
                for (int j = 0; j < 4; ++j) { a[2 * j] += sc[i] * lo16(e1[i][j]); a[2 * j + 1] += sc[i] * hi16(e1[i][j]); }
            const float one = (L >> 6) == 2 ? 1.f : 0.f;
#pragma unroll
            for (int i = 0; i < 8; ++i) a[i] = bfr(wave_sum(a[i])) + one;
            g1a = (l & 1) ? v4u{pk(a[4], a[5]), tag, pk(a[6], a[7]), tag} : v4u{pk(a[0], a[1]), tag, pk(a[2], a[3]), tag};
        }
    }
    STAMP(2);

    float xh[8]; v2u xc0, xc1; float mean1;
    {
        lds<v4u>(L_XS + w * 1024 + l * 16) = xv;
        float v[8];
#pragma unroll
        for (int i = 0; i < 4; ++i) { v[2 * i] = lo16(xv[i]); v[2 * i + 1] = hi16(xv[i]); }
        mean1 = wave_sum(((v[0] + v[1]) + (v[2] + v[3])) + ((v[4] + v[5]) + (v[6] + v[7]))) * (1.f / 512);
        float q = 0.f;
#pragma unroll
        for (int i = 0; i < 8; ++i) q += (v[i] - mean1) * (v[i] - mean1);
        const float rstd = __builtin_amdgcn_rsqf(wave_sum(q) * (1.f / 512) + 1e-5f);
        asm volatile("s_waitcnt lgkmcnt(0)" ::: "memory");
        const v4u xa = ldtr2(L_XS + w * 1024 + 8 * l, L_XS + w * 1024 + 512 + 8 * l);
        xc0 = lds<v2u>(L_XS + w * 1024 + 64 * li + 8 * g); xc1 = lds<v2u>(L_XS + w * 1024 + 64 * li + 32 + 8 * g);
#pragma unroll
        for (int i = 0; i < 4; ++i) { xh[2 * i] = (lo16(xa[i]) - mean1) * rstd; xh[2 * i + 1] = (hi16(xa[i]) - mean1) * rstd; }
        v4u wp;
#pragma unroll
        for (int r = 0; r < 4; ++r) wp[r] = wre[r] | (wim[r] << 16);
        const float h0r = lo16(wre[0]) + lo16(wx[2]), h0i = lo16(wim[0]) - lo16(wx[3]);
        const float h1r = lo16(wx[0]) + lo16(wx[4]), h1i = lo16(wx[1]) - lo16(wx[5]);
        if (l == 0) {
            wp[0] = pk(0.25f * (h0r + h1r), 0.25f * (h0i + h1i));
            lds<u32>(L_T0 + w * 1040 + 1024) = pk(0.25f * (h0r - h1r), 0.25f * (h0i - h1i));
        }
        lds<v4u>(L_T0 + w * 1040 + l * 16) = wp;
    }
    BAR();
    const u32 trd = (4 * (t & 1)) * 1040 + (t >> 1) * 4;
    {
        v4u o;
#pragma unroll
        for (int j = 0; j < 4; ++j) o[j] = lds<u32>(L_T0 + trd + j * 1040);
        st128c(o, rws, WS_WT + L * XSTR + t * 16);
        if (t < 2) {
#pragma unroll
            for (int j = 0; j < 4; ++j) o[j] = lds<u32>(L_T0 + trd + j * 1040 + 1024);
            st128c(o, rws, WS_WT + L * XSTR + 8192 + t * 16);
        }
    }
    asm volatile("" ::"v"(cpf));
    STAMP(3);
    const v4u rs1 = mkrs4(pw1, 512 * 1792 * 2), rs3 = mkrs4(pw3, 512 * 1792 * 2);
    const u32 wvoff = ((64 * w + l) / 14) * 3584 + ((64 * w + l) % 14) * 16 + mem * 224;
    VM0();
    if (w < 7) {
#pragma unroll
        for (int p = 0; p < 8; ++p) { dma16(rs1, wvoff, p * 114688, L_W1 + p * 7168 + w * 1024); dma16(rs3, wvoff, p * 114688, L_W3 + p * 7168 + w * 1024); }
    } else {
        nap<D_E0>();
        v4u m[4];
#define G0_LOAD do { _Pragma("unroll") for (int i = 0; i < 4; ++i) m[i] = ld128c(rws, WS_G0 + i * 1024 + l * 16); } while (0)
        G0_LOAD;
        RETRY((m[0].y != tag) | (m[0].w != tag) | (m[1].y != tag) | (m[1].w != tag) | (m[2].y != tag) | (m[2].w != tag) | (m[3].y != tag) | (m[3].w != tag), G0_LOAD);
#pragma unroll
        for (int i = 0; i < 4; ++i) lds<v2u>(L_MOD0 + i * 512 + l * 8) = v2u{m[i].x, m[i].z};
        if (L == 0 && l == 0) __builtin_amdgcn_raw_buffer_store_b32(tag, rws, WS_EPOCH, 0, SC1);
    }
    BAR();
    STAMP(4);
    if (w == 6 && l < 2) st128c(g1a, rws, WS_G1 + 32 * L + 16 * l);

    {
        const v4u sa = ldtr2(L_MOD0 + 8 * l, L_MOD0 + 512 + 8 * l), ca = ldtr2(L_MOD0 + 1024 + 8 * l, L_MOD0 + 1536 + 8 * l);
        v4u ha;
#pragma unroll
        for (int i = 0; i < 4; ++i) ha[i] = pk(xh[2 * i] * lo16(ca[i]) + lo16(sa[i]), xh[2 * i + 1] * hi16(ca[i]) + hi16(sa[i]));
        const f4 yr = mfma16(ha, aBR, z4), yi = mfma16(ha, aBI, z4);
        const v4u yb = twid_pack(yr, yi, tc, ts);
        const f4 xr = mfma16(FR, yb, z4), xi = mfma16(aAI, yb, z4), sr = mfma16(aSR, yb, z4), si = mfma16(aSI, yb, z4);
        v4u o;
#pragma unroll
        for (int r = 0; r < 4; ++r) o[r] = li ? pk(xr[r], xi[r]) : pk(sr[r], si[r]);
        lds<v4u>(L_T1 + w * 1040 + l * 16) = o;
        BAR();
#pragma unroll
        for (int j = 0; j < 4; ++j) o[j] = lds<u32>(L_T1 + trd + j * 1040);
        st128c(o, rws, WS_X1 + L * XSTR + t * 16);
    }
    STAMP(5);
    {
        v4u m = ld128c(rws, WS_G1 + t * 16);
        RETRY((m.y != tag) | (m.w != tag), m = ld128c(rws, WS_G1 + t * 16));
        lds<v2u>(L_MOD1 + t * 8) = v2u{m.x, m.z};
    }
    VM0();
    BAR();
    STAMP(6);
    nap<D_E1>();
    const u32 xrd = (t >> 1) * XSTR + L * 32 + (t & 1) * 16;
    v4u zin = ld128c(rws, WS_X1 + xrd);
    const v4u wf4 = ld128c(rws, WS_WT + xrd);
    v4u wb4 = {0, 0, 0, 0};
    if (L == 0) wb4 = ld128c(rws, WS_WT + xrd + 8192);
    SCHED();
    v4u wf[32];
    const u32 wtr = ((l >> 4) & 1) * L_W3 + (4 * (l >> 5) + ((l >> 2) & 3)) * 224 + w * 32 + (((l & 1) << 1) | ((l >> 1) & 1)) * 8;
#pragma unroll
    for (int u = 0; u < 16; ++u) { const u32 o = wtr + (32 * (u >> 1) + 8 * (u & 1)) * 224; wf[u] = ldtr2(o, o + 16 * 224); }
    BAR();
    RETRY(vmax(zin, 0) == SENT, zin = ld128c(rws, WS_X1 + xrd));
    STAMP(7);

    {
        f4 re = mfma16(zin, FR, z4), im = mfma16(zin, FI, z4);
        v4u zb = twid_pack(re, im, bc, bs);
        re = mfma16(GR, zb, z4); im = mfma16(GI, zb, z4);
        v4u o;
#pragma unroll
        for (int r = 0; r < 4; ++r) o[r] = pk(re[r], im[r]);
        lds<v4u>(L_E1 + w * 1088 + l * 16) = o;
        BAR();
        const u32 eo = 2 * g * 1088 + (32 * w + 2 * li) * 4;
        const v2u u0 = lds<v2u>(L_E1 + eo), u1 = lds<v2u>(L_E1 + eo + 1088);
        f4 d[2] = {mfma16k(A8, v2u{u0.x, u1.x}, z4), mfma16k(A8, v2u{u0.y, u1.y}, z4)};
        f4 f[2];
#pragma unroll
        for (int m = 0; m < 2; ++m)
#pragma unroll
            for (int b = 0; b < 2; ++b) {
                const float wr = lo16(wf4[2 * m + b]), wi = hi16(wf4[2 * m + b]), zr = d[m][2 * b], zi = d[m][2 * b + 1];
                f[m][2 * b] = zr * wr - zi * wi; f[m][2 * b + 1] = zr * wi + zi * wr;
            }
        if (L == 0) {
            v2u A1, A0;
#pragma unroll
            for (int jb = 0; jb < 2; ++jb) {
                const float a = (float)((2 * g + jb) * ((li >> 1) + 1)) * 0.125f, c = cosr(a), s = sinr(a);
                A1[jb] = (li & 1) ? pk(-s, -c) : pk(c, -s);
                A0[jb] = A8[jb] ^ 0x80000000u;
            }
#pragma unroll
            for (int m = 0; m < 2; ++m) {
                const int pos = 32 * w + 2 * li + m, q = ((pos >> 2) & 15) + 16 * (4 * (pos >> 6) + (pos & 3)), qm = (256 - q) & 255;
                const int pm = 64 * (qm >> 6) + 4 * (qm & 15) + ((qm >> 4) & 3);
                const v2u um = {lds<u32>(L_E1 + 2 * g * 1088 + pm * 4), lds<u32>(L_E1 + (2 * g + 1) * 1088 + pm * 4)};
                const f4 m1 = mfma16k(A1, um, z4), m0 = mfma16k(A0, um, z4);
#pragma unroll
                for (int b = 0; b < 2; ++b) {
                    const float wr = lo16(wb4[2 * m + b]), wi = hi16(wb4[2 * m + b]);
                    const float zr = q ? m1[2 * b] : m0[2 * b], zi = q ? m1[2 * b + 1] : m0[2 * b + 1];
                    f[m][2 * b] += zr * wr - zi * wi; f[m][2 * b + 1] += zr * wi + zi * wr;
                }
            }
        }
        d[0] = mfma16k(A8, v2u{pk(f[0][1], f[0][0]), pk(f[0][3], f[0][2])}, z4);
        d[1] = mfma16k(A8, v2u{pk(f[1][1], f[1][0]), pk(f[1][3], f[1][2])}, z4);
        lds<v2u>(L_E2 + eo) = v2u{pk(d[0][0], d[0][1]), pk(d[1][0], d[1][1])};
        lds<v2u>(L_E2 + eo + 1088) = v2u{pk(d[0][2], d[0][3]), pk(d[1][2], d[1][3])};
        BAR();
        zb = lds<v4u>(L_E2 + w * 1088 + l * 16);
        re = mfma16(zb, HR, z4); im = mfma16(zb, HI, z4);
        zb = twid_pack(re, im, bc2, bs2);
        re = mfma16(FR, zb, z4); im = mfma16(FI, zb, z4);
#pragma unroll
        for (int r = 0; r < 4; ++r) o[r] = pk(im[r], re[r]);
        st128c(o, rws, WS_X2 + L * XSTR + t * 16);
    }
    STAMP(8);
    if (w < 7) {
#pragma unroll
        for (int p = 0; p < 8; ++p) { dma16(rs1, wvoff, (p + 8) * 114688, L_W1 + p * 7168 + w * 1024); dma16(rs3, wvoff, (p + 8) * 114688, L_W3 + p * 7168 + w * 1024); }
    }
    VM0();
    BAR();
    STAMP(9);
    nap<D_E2>();
    zin = ld128c(rws, WS_X2 + xrd);
    SCHED();
#pragma unroll
    for (int u = 0; u < 16; ++u) { const u32 o = wtr + (32 * (u >> 1) + 8 * (u & 1)) * 224; wf[16 + u] = ldtr2(o, o + 16 * 224); }
    v4u cBR, cBI, cSR, cSI, cB0, cB1;
    float ga[8], sb[8], cb[8];
    {
        const float sg = g < 2 ? 1.f : -1.f;
#pragma unroll
        for (int r = 0; r < 4; ++r) {
            const float kk = fg4 + r, a = kk * fl * (1.f / 16), c = cosr(a), s = sinr(a);
            cBR[r] = pk(c, -sg * s); cBI[r] = pk(s, sg * c);
            const float a2 = g < 2 ? a : (2.f * kk - 15.f) * fl * (1.f / 32);
            u32 v = pk(2.f * cosr(a2), -2.f * sinr(a2));
            if (r == 0) v = g == 0 ? pk(1.f, (li & 1) ? -1.f : 1.f) : v;
            cSR[r] = g < 2 ? v : 0u; cSI[r] = g < 2 ? 0u : v;
            const float p0 = 2.f * fl * kk * (1.f / 32), p1 = (2.f * fl + 1.f) * kk * (1.f / 32);
            u32 v0 = pk(2.f * cosr(p0), -2.f * sinr(p0)), v1 = pk(2.f * cosr(p1), -2.f * sinr(p1));
            if (r == 0) { v0 = g == 0 ? pk(1.f, 1.f) : v0; v1 = g == 0 ? pk(1.f, -1.f) : v1; }
            cB0[r] = v0; cB1[r] = v1;
        }
#pragma unroll
        for (int h = 0; h < 2; ++h) {
            const u32 o = L_MOD1 + 64 * li + 32 * h + 8 * g;
            const v2u a = lds<v2u>(o), b = lds<v2u>(o + 1024), c = lds<v2u>(o + 2048);
#pragma unroll
            for (int i = 0; i < 2; ++i) {
                ga[4 * h + 2 * i] = lo16(a[i]) * (1.f / 1048576); ga[4 * h + 2 * i + 1] = hi16(a[i]) * (1.f / 1048576);
                sb[4 * h + 2 * i] = lo16(b[i]); sb[4 * h + 2 * i + 1] = hi16(b[i]);
                cb[4 * h + 2 * i] = lo16(c[i]); cb[4 * h + 2 * i + 1] = hi16(c[i]);
            }
        }
        PIN(cBR); PIN(cBI); PIN(cSR); PIN(cSI); PIN(cB0); PIN(cB1);
#pragma unroll
        for (int i = 0; i < 8; ++i) { PIN(ga[i]); PIN(sb[i]); PIN(cb[i]); }
    }
    SCHED();
    RETRY(vmax(zin, 0) == SENT, zin = ld128c(rws, WS_X2 + xrd));
    STAMP(10);

    {
#pragma unroll
        for (int j = 0; j < 4; ++j) lds<u32>(L_T0 + trd + j * 1040) = zin[j];
        BAR();
        const v4u va = lds<v4u>(L_T0 + w * 1040 + l * 16);
        f4 ur = mfma16(va, cBR, z4), ui = mfma16(va, cBI, z4);
        const f4 sr = mfma16(va, cSR, z4), si = mfma16(va, cSI, z4);
        if (g == 0) { ur[0] = sr[0]; ui[0] = si[0]; }
        v4u ta;
#pragma unroll
        for (int r = 0; r < 4; ++r) ta[r] = pk(ur[r] * tc[r] - ui[r] * ts[r], ui[r] * tc[r] + ur[r] * ts[r]);
        const f4 y0 = mfma16(ta, cB0, z4), y1 = mfma16(ta, cB1, z4);
        float v[8];
        v[0] = lo16(xc0.x); v[1] = hi16(xc0.x); v[2] = lo16(xc0.y); v[3] = hi16(xc0.y);
        v[4] = lo16(xc1.x); v[5] = hi16(xc1.x); v[6] = lo16(xc1.y); v[7] = hi16(xc1.y);
        float s1 = 0.f, s2 = 0.f;
#pragma unroll
        for (int i = 0; i < 8; ++i) {
            v[i] = (v[i] - mean1) + ga[i] * (i < 4 ? y0[i & 3] : y1[i & 3]);
            s1 += v[i]; s2 += v[i] * v[i];
        }
        const float m2 = wave_sum(s1) * (1.f / 512);
        const float rstd = __builtin_amdgcn_rsqf(fmaxf(wave_sum(s2) * (1.f / 512) - m2 * m2, 0.f) + 1e-5f);
        v4u ho, xo;
#pragma unroll
        for (int i = 0; i < 4; ++i) {
            ho[i] = pk((v[2 * i] - m2) * rstd * cb[2 * i] + sb[2 * i], (v[2 * i + 1] - m2) * rstd * cb[2 * i + 1] + sb[2 * i + 1]);
            xo[i] = pk(v[2 * i] + mean1, v[2 * i + 1] + mean1);
        }
        st128c(ho, rws, WS_H2 + h2_w);
        st128c(xo, rws, WS_XR + h2_w);
    }
    STAMP(11);
    VM0();
    nap<D_E3>();

#define A_OFF(n) (((n) >> 1) * 32768 + ((n) & 1) * 512)
#define A_DECL const int l = fresh_lane(); \
        const u32 a_rd = WS_H2 + (P * 128 + 2 * w + (l >> 5)) * 1024 + (l & 31) * 16, a_wr = L_RING + (l & 31) * 528 + (2 * w + (l >> 5)) * 16; \
        const u32 v2a = (l >> 3) * 1024 + ct * 128 + ((((l >> 1) & 3) ^ ((l >> 4) & 1)) * 2 + (l & 1)) * 16, v2b = v2a ^ 64; \
        v4u sa0[2], sa1[2], w2f[28]; u32 w2a[4]
#define A_LOAD(n) do { sa0[n] = ld128c(rws, a_rd, A_OFF(n)); sa1[n] = ld128c(rws, a_rd, A_OFF(n) + 16384); } while (0)
#define A_PUT(n) do { RETRY(vmax(sa1[n], vmax(sa0[n], 0)) == SENT, A_LOAD(n)); \
        lds<v4u>(a_wr + (n) * SLOT) = sa0[n]; lds<v4u>(a_wr + (n) * SLOT + 256) = sa1[n]; } while (0)
    const v4u rs2 = mkrs4(pw2, 1792 * 512 * 2);
    const int ct = mem & 7, rp = mem >> 3;
#define W2_ADDR(base) do { const int l_ = fresh_lane(); _Pragma("unroll") for (int nt = 0; nt < 4; ++nt) { \
        w2a[nt] = (base) + (8 * (l_ >> 4) + ((l_ >> 2) & 3)) * 128 + (l_ & 3) * 8 + ((nt ^ ((l_ >> 3) & 3)) * 32); PIN(w2a[nt]); } } while (0)
#define W2_FRAG(slot, nt) ldtr2(w2a[nt] + (slot) * 4096, w2a[nt] + (slot) * 4096 + 512)
#define W2_DMA(ks, i, base) dma16(rs2, ((i) & 1) ? v2b : v2a, (w * 224 + (ks) * 32 + (i) * 8) * 1024, (base) + (i) * 1024)
    auto gemm2 = [&](const v4u (&w2f)[28]) __attribute__((always_inline)) {
    STAMP(13);

    {
        const int l = fresh_lane(), li = l & 15, g = l >> 4;
        const u32 h_rd = WS_HH + (P * 8 + rp) * 57344 + w * 7168 + l * 16;
        v4u hb[3][7];
#define H_LOAD(q) do { _Pragma("unroll") for (int k = 0; k < 7; ++k) hb[(q) % 3][k] = ld128c(rws, h_rd + k * 1024, (q) * 114688); } while (0)
        H_LOAD(0); H_LOAD(1); H_LOAD(2);
        SCHED();
#pragma unroll
        for (int q = 0; q < 4; ++q) {
            RETRY(vmaxn<7>(hb[q % 3]) == SENT, H_LOAD(q));
            f4 o[4] = {z4, z4, z4, z4};
#pragma unroll
            for (int k = 0; k < 7; ++k)
#pragma unroll
                for (int nt = 0; nt < 4; ++nt) o[nt] = mfma16(w2f[4 * k + nt], hb[q % 3][k], o[nt]);
            if (q == 0) H_LOAD(3);
#pragma unroll
            for (int nt = 0; nt < 4; ++nt) lds<f4>(w * 16384 + q * 4096 + nt * 1024 + l * 16) = o[nt];
            SCHED();
        }
        STAMP(14);
        BAR();
        const int q = w >> 1, cg = (w & 1) * 4 + g, pr = (2 * q + rp) * 16 + li;
        const u32 xo = WS_XR + (P * 128 + pr) * 1024 + (4 * (2 * ct + (cg >> 2)) + 2 * (cg & 1)) * 16 + ((cg >> 1) & 1) * 8;
        const v2u xa = ld64c(rws, xo), xb = ld64c(rws, xo + 16);
        const v4u gbv = lds<v4u>(L_MOD1 + 3072 + ct * 128 + cg * 16);
        f4 s0 = z4, s1 = z4;
        const u32 po = q * 4096 + (cg >> 1) * 1024 + (cg & 1) * 512 + li * 16;
#pragma unroll
        for (int i = 0; i < 8; ++i) { s0 += lds<f4>(po + i * 16384); s1 += lds<f4>(po + i * 16384 + 256); }
        const int m2 = pr >> 3, L2 = (P >> 2) * 64 + (m2 >> 3) * 32 + (P & 3) * 8 + (m2 & 7);
        const v4u o = {pk(lo16(xa.x) + lo16(gbv.x) * s0[0], hi16(xa.x) + hi16(gbv.x) * s0[1]), pk(lo16(xa.y) + lo16(gbv.y) * s0[2], hi16(xa.y) + hi16(gbv.y) * s0[3]),
                       pk(lo16(xb.x) + lo16(gbv.z) * s1[0], hi16(xb.x) + hi16(gbv.z) * s1[1]), pk(lo16(xb.y) + lo16(gbv.w) * s1[2], hi16(xb.y) + hi16(gbv.w) * s1[3])};
        __builtin_amdgcn_raw_buffer_store_b128(o, mkrs(pout, 2048 * 1024), row_of(L2, pr & 7) * 1024 + ct * 128 + cg * 16, 0, 0);
    }
    STAMP(15);
    };
    int w_ = w; asm volatile("" : "+s"(w_));
    if (w < 7) {
        A_DECL;
        A_LOAD(0); A_LOAD(1);
        BAR();
        A_PUT(0);
        BAR();
        STAMP(12);
        const u32 b_rd = L_RING + (l >> 5) * 528 + (l & 31) * 16;
        v4u bf[4];
        f16v acc[2];
#pragma unroll
        for (int u = 0; u < 4; ++u) bf[u] = lds<v4u>(b_rd + u * 1056);
#pragma unroll
        for (int n = 0; n < 8; ++n) {
            const int q = n >> 1, kh = n & 1;
            float hv[8];
#pragma unroll
            for (int u = 0; u < 16; ++u) {
                f16v cin = acc[q & 1];
                if (kh == 0 && u == 0) cin = f16v{0.f, 0.f, 0.f, 0.f, 0.f, 0.f, 0.f, 0.f, 0.f, 0.f, 0.f, 0.f, 0.f, 0.f, 0.f, 0.f};
                acc[q & 1] = mfma32(wf[16 * kh + u], bf[u & 3], cin);
                if (u < 12) bf[u & 3] = lds<v4u>(b_rd + kh * SLOT + (u + 4) * 1056);
                if (u == 13) {
                    BAR();
                    if (n < 7) { bf[0] = lds<v4u>(b_rd + (kh ^ 1) * SLOT); bf[1] = lds<v4u>(b_rd + (kh ^ 1) * SLOT + 1056); }
                }
                if (u >= 14 && n < 7) bf[u & 3] = lds<v4u>(b_rd + (kh ^ 1) * SLOT + (u - 12) * 1056);
                if (u == 1 && n == 0) A_PUT(1);
                if (kh == 0 && q > 0) {
                    if (u >= 2 && u < 10) hv[u - 2] = silu(acc[(q - 1) & 1][u - 2]) * acc[(q - 1) & 1][u + 6];
                    if (u == 10) st128c(v4u{pk(hv[0], hv[1]), pk(hv[2], hv[3]), pk(hv[4], hv[5]), pk(hv[6], hv[7])}, rws, hh_w, (q - 1) * 114688);
                }
                if (n == 0 && u >= 4 && u < 12) W2_DMA((u - 4) >> 2, u & 3, w * 16384 + ((u - 4) >> 2) * 4096);
                if (n == 1 && u >= 4 && u < 12) W2_DMA(2 + ((u - 4) >> 2), u & 3, w * 16384 + (2 + ((u - 4) >> 2)) * 4096);
                if (n == 3 && u == 0) { VM0(); W2_ADDR(w * 16384); }
                if (n == 3 && u < 12) w2f[u] = W2_FRAG(u >> 2, u & 3);
                if (n == 4 && u < 12 && (u & 1)) W2_DMA(4 + (u >> 3), (u >> 1) & 3, w * 16384 + (u >> 3) * 4096);
                if (n == 5 && u < 12 && (u & 1)) W2_DMA(4 + ((u + 12) >> 3), ((u + 12) >> 1) & 3, w * 16384 + ((u + 12) >> 3) * 4096);
                if (n == 7 && u == 0) { VM0(); W2_ADDR(w * 16384); }
                if (n == 7) w2f[12 + u] = W2_FRAG(((u >> 2) + 3) & 3, u & 3);
                SCHED();
            }
        }
        float hv[8];
#pragma unroll
        for (int r = 0; r < 8; ++r) hv[r] = silu(acc[1][r]) * acc[1][r + 8];
        st128c(v4u{pk(hv[0], hv[1]), pk(hv[2], hv[3]), pk(hv[4], hv[5]), pk(hv[6], hv[7])}, rws, hh_w, 3 * 114688);
        SCHED();
        gemm2(w2f);
    }
    if (w_ == 7) {
        A_DECL;
        A_LOAD(0); A_LOAD(1);
        const u32 s_rd = WS_H2 + (P * 128 + (l >> 5)) * 1024 + (l & 31) * 16, s_wr = L_RING + (l & 31) * 528 + (l >> 5) * 16;
        v4u st[16];
#define S_LOAD(n, i) st[i] = ld128c(rws, s_rd, A_OFF(n) + (i) * 2048)
#define S_LOADALL(n) do { _Pragma("unroll") for (int i = 0; i < 16; ++i) S_LOAD(n, i); } while (0)
        S_LOADALL(2);
        W2_ADDR(L_B7);
        BAR();
        A_PUT(0);
        BAR();
        STAMP(12);
        A_PUT(1);
#pragma unroll
        for (int n = 0; n < 8; ++n) {
            if (n >= 1 && n < 7) {
                RETRY(vmaxn<16>(st) == SENT, S_LOADALL(n + 1));
                VM0();
#pragma unroll
                for (int i = 0; i < 16; ++i) { lds<v4u>(s_wr + ((n + 1) & 1) * SLOT + i * 32) = st[i]; if (n < 6) S_LOAD(n + 2, i); }
            }
            if (n >= 1 && n < 5) {
#pragma unroll
                for (int u = 0; u < 8; ++u) if (8 * (n - 1) + u < 28) w2f[8 * (n - 1) + u] = W2_FRAG(u >> 2, u & 3);
                asm volatile("s_waitcnt lgkmcnt(0)" ::: "memory");
            }
            if (n < 4) {
#pragma unroll
                for (int u = 0; u < 8; ++u) if (8 * n + u < 28) W2_DMA(2 * n + (u >> 2), u & 3, L_B7 + (u >> 2) * 4096);
            }
            BAR();
            SCHED();
        }
        gemm2(w2f);
        asm volatile(".Lcode_tail:");
    }
}

extern "C" void kernel_launch(void* const* d_in, const int* in_sizes, int n_in, void* d_out, int out_size, void* d_ws, size_t ws_size,
                              hipStream_t stream) {
    (void)in_sizes; (void)n_in; (void)out_size; (void)ws_size;
    (void)hipFuncSetAttribute((const void*)fused_kernel, hipFuncAttributeMaxDynamicSharedMemorySize, L_TOTAL);
    fused_kernel<<<256, 512, L_TOTAL, stream>>>((const u16*)d_in[0], (const u16*)d_in[1], (const u16*)d_in[2], (const u16*)d_in[4],
                                                (const u16*)d_in[5], (const u16*)d_in[6], (const u16*)d_in[7], (const u16*)d_in[8],
                                                (u16*)d_out, (unsigned char*)d_ws);
}
```

```cpp
#include <hip/hip_runtime.h>
#include <hip/hip_bf16.h>

typedef unsigned u32;
typedef unsigned short u16;
typedef u32 v4u __attribute__((ext_vector_type(4)));
typedef u32 v2u __attribute__((ext_vector_type(2)));
typedef float f2 __attribute__((ext_vector_type(2)));
typedef float f4 __attribute__((ext_vector_type(4)));
typedef float f16v __attribute__((ext_vector_type(16)));
typedef short s4 __attribute__((ext_vector_type(4)));
typedef __bf16 b8 __attribute__((ext_vector_type(8)));
typedef __bf16 b2 __attribute__((ext_vector_type(2)));
typedef __amdgpu_buffer_rsrc_t rsrc_t;
#define LAS __attribute__((address_space(3)))
#define DEV __device__ __forceinline__

constexpr u32 WS_EPOCH = 0;
constexpr u32 WS_TK = 1024;
constexpr u32 WS_FL = 2048;
constexpr u32 WS_G0 = 4096;
constexpr u32 WS_G1 = 8192;
constexpr u32 XSTR = 8320;
constexpr u32 WS_X1 = 65536;
constexpr u32 WS_WT = WS_X1 + 256 * XSTR;
constexpr u32 WS_X2 = WS_WT + 256 * XSTR;
constexpr u32 WS_H2 = WS_X2 + 256 * XSTR;
constexpr u32 WS_XR = WS_H2 + 2048 * 1024;
constexpr u32 WS_HH = WS_XR + 2048 * 1024;
constexpr u32 WS_END = WS_HH + 2048 * 1792 * 2;
constexpr u32 WS_DBG = WS_END;

constexpr u32 SLOT = 16896;
constexpr u32 L_XS = 0;
constexpr u32 L_T0 = 8192;
constexpr u32 L_T1 = L_T0 + 8320;
constexpr u32 L_WR = L_T0;
constexpr u32 L_AB = L_WR + 8960;
constexpr u32 L_E1 = L_XS;
constexpr u32 L_E2 = L_T1 + 8320;
constexpr u32 L_MOD0 = 2 * SLOT;
constexpr u32 L_MOD1 = L_MOD0 + 2048;
constexpr u32 L_BIG = 40960;
constexpr u32 L_TOTAL = L_BIG + 7 * 16384;
static_assert(L_E2 + 8 * 1088 <= L_MOD0 && L_MOD1 + 4096 <= L_BIG && L_TOTAL <= 163840, "lds");

constexpr u32 SENT = 0xFFFFFFFFu;
constexpr u32 SPIN_CAP = 1u << 16;
constexpr int SC1 = 16;
#define NT 2
#define D_E0 8
#define D_E1 0
#define D_E2 0
#define D_E3 0
#define NPA 2
#define NPB 2
#define NPC 1
#define NPD 5
#define NPG 5
constexpr int NB1 = 1 + NPA + NPB + NPC, NB2 = NPD, NB3 = NPG;
static_assert(NB1 + NB2 + NB3 == 16 && NB1 <= 7 && NB2 <= 7 && NB3 <= 7, "schedule");

__shared__ __attribute__((aligned(16))) unsigned char smem[L_TOTAL];

DEV u32 pk(float lo, float hi) { f2 v = {lo, hi}; return __builtin_bit_cast(u32, __builtin_convertvector(v, b2)); }
DEV float lo16(u32 x) { return __builtin_bit_cast(float, x << 16); }
DEV float hi16(u32 x) { return __builtin_bit_cast(float, x & 0xFFFF0000u); }
DEV float bfr(float x) { return lo16(pk(x, x)); }
DEV float cosr(float rev) { return __builtin_amdgcn_cosf(rev); }
DEV float sinr(float rev) { return __builtin_amdgcn_sinf(rev); }
DEV f4 mfma16(v4u a, v4u b, f4 c) { return __builtin_amdgcn_mfma_f32_16x16x32_bf16(__builtin_bit_cast(b8, a), __builtin_bit_cast(b8, b), c, 0, 0, 0); }
DEV f4 mfma16k(v2u a, v2u b, f4 c) { return __builtin_amdgcn_mfma_f32_16x16x16bf16_1k(__builtin_bit_cast(s4, a), __builtin_bit_cast(s4, b), c, 0, 0, 0); }
DEV f16v mfma32(v4u a, v4u b, f16v c) { return __builtin_amdgcn_mfma_f32_32x32x16_bf16(__builtin_bit_cast(b8, a), __builtin_bit_cast(b8, b), c, 0, 0, 0); }
DEV v2u ldtr(u32 off) { return __builtin_bit_cast(v2u, __builtin_amdgcn_ds_read_tr16_b64_v4i16((LAS s4*)(smem + off))); }
DEV v4u ldtr2(u32 off0, u32 off1) { v2u a = ldtr(off0), b = ldtr(off1); return v4u{a.x, a.y, b.x, b.y}; }
template <class T> DEV T& lds(u32 off) { return *(T*)(smem + off); }
DEV rsrc_t mkrs(const void* p, u32 bytes) { return __builtin_amdgcn_make_buffer_rsrc((void*)p, 0, bytes, 0x00020000); }
DEV v4u mkrs4(const void* p, u32 bytes) { unsigned long long a = (unsigned long long)p; return v4u{(u32)a, (u32)(a >> 32) & 0xFFFFu, bytes, 0x00020000u}; }
DEV v4u ld128(rsrc_t r, u32 off) { return __builtin_amdgcn_raw_buffer_load_b128(r, off, 0, 0); }
DEV v4u ld128n(rsrc_t r, u32 off, u32 soff = 0) { return __builtin_amdgcn_raw_buffer_load_b128(r, off, soff, NT); }
DEV v2u ld64n(rsrc_t r, u32 off, u32 soff = 0) { return __builtin_amdgcn_raw_buffer_load_b64(r, off, soff, NT); }
DEV v2u ld64(rsrc_t r, u32 off) { return __builtin_amdgcn_raw_buffer_load_b64(r, off, 0, 0); }
DEV u32 ld16(rsrc_t r, u32 off) { return (u32)__builtin_amdgcn_raw_buffer_load_b16(r, off, 0, 0); }
DEV v4u ld128c(rsrc_t r, u32 off, u32 soff = 0) { return __builtin_amdgcn_raw_buffer_load_b128(r, off, soff, SC1); }
DEV v2u ld64c(rsrc_t r, u32 off) { return __builtin_amdgcn_raw_buffer_load_b64(r, off, 0, SC1); }
DEV void st128c(v4u v, rsrc_t r, u32 off, u32 base = 0) { __builtin_amdgcn_raw_buffer_store_b128(v, r, off + base, 0, SC1); }
DEV void dma16(v4u rs, u32 voff, u32 soff, u32 ldsa) {
    asm volatile("s_mov_b32 m0, %3\n\ts_nop 0\n\tbuffer_load_dwordx4 %0, %1, %2 offen lds" ::"v"(voff), "s"(rs), "s"(soff), "s"(ldsa) : "memory");
}
DEV void ld128a(v4u& d, v4u rs, u32 voff) { asm volatile("buffer_load_dwordx4 %0, %1, %2, 0 offen sc1" : "=v"(d) : "v"(voff), "s"(rs) : "memory"); }
template <int N> DEV void waitv(v4u& d) { asm volatile("s_waitcnt vmcnt(%1)" : "+v"(d) : "n"(N) : "memory"); }
template <int N> DEV void waitvm() { asm volatile("s_waitcnt vmcnt(%0)" ::"n"(N) : "memory"); }
#define VM0() asm volatile("s_waitcnt vmcnt(0)" ::: "memory")
#define BAR() do { asm volatile("s_waitcnt lgkmcnt(0)" ::: "memory"); __builtin_amdgcn_s_barrier(); asm volatile("" ::: "memory"); } while (0)
#define SCHED() __builtin_amdgcn_sched_barrier(0)
#define PIN(x) asm volatile("" : "+v"(x))
#define RETRY(BAD, LOAD) do { if (__builtin_expect(anylane(BAD), 0)) { u32 sp_ = 0; \
        _Pragma("nounroll") do { asm volatile("" ::: "memory"); LOAD; } while (anylane(BAD) && ++sp_ < SPIN_CAP); } } while (0)
template <int N> DEV void nap() { if (N > 0) __builtin_amdgcn_s_sleep(N); }
DEV u32 umax3(u32 a, u32 b, u32 c) { return max(max(a, b), c); }
DEV u32 vmax(v4u v, u32 m) { return umax3(umax3(m, v.x, v.y), v.z, v.w); }
template <int N> DEV u32 vmaxn(const v4u (&v)[N]) { u32 m = 0;
#pragma unroll
    for (int i = 0; i < N; ++i) m = vmax(v[i], m);
    return m; }
DEV int fresh_lane() { int x; asm volatile("v_mbcnt_lo_u32_b32 %0, -1, 0\n\tv_mbcnt_hi_u32_b32 %0, -1, %0" : "=v"(x)); return x; }
DEV bool anylane(bool b) { return __builtin_amdgcn_ballot_w64(b) != 0ull; }

template <int CTRL, int RM> DEV float dppadd(float v) {
    return v + __builtin_bit_cast(float, __builtin_amdgcn_update_dpp(0, __builtin_bit_cast(int, v), CTRL, RM, 0xF, true));
}
DEV float wave_sum(float v) {
    v = dppadd<0xB1, 0xF>(v); v = dppadd<0x4E, 0xF>(v); v = dppadd<0x141, 0xF>(v); v = dppadd<0x140, 0xF>(v);
    v = dppadd<0x142, 0xF>(v); v = dppadd<0x143, 0xF>(v);
    return __builtin_bit_cast(float, __builtin_amdgcn_readlane(__builtin_bit_cast(int, v), 63));
}
DEV float silu(float x) { return x * __builtin_amdgcn_rcpf(1.0f + __builtin_amdgcn_exp2f(-1.44269504089f * x)); }

DEV void fwd_pair(float rev0, float drev, v4u& R, v4u& I) {
#pragma unroll
    for (int r = 0; r < 4; ++r) { const float a = rev0 + r * drev, c = cosr(a), s = sinr(a); R[r] = pk(c, s); I[r] = pk(-s, c); }
}
DEV v4u pk4(f4 a, f4 b) { return v4u{pk(a[0], b[0]), pk(a[1], b[1]), pk(a[2], b[2]), pk(a[3], b[3])}; }
DEV v4u twid_pack(f4 re, f4 im, const f4& c, const f4& s) {
    f4 a = re * c + im * s, b = im * c - re * s;
    PIN(a); PIN(b);
    return pk4(a, b);
}
DEV int row_of(int L, int rho) { return 128 * (4 * ((L >> 3) & 3) + (rho & 3)) + 8 * (2 * (L & 7) + (rho >> 2)) + (L >> 5); }

#define STAMP(i)
#define STAMPC(i)
#define STAMP_FLUSH()

__global__ void __launch_bounds__(512, 2)
fused_kernel(const u16* __restrict__ px, const u16* __restrict__ pwr, const u16* __restrict__ pwi, const u16* __restrict__ pw1,
             const u16* __restrict__ pw3, const u16* __restrict__ pw2, const u16* __restrict__ pc, const u16* __restrict__ pwm,
             u16* __restrict__ pout, unsigned char* __restrict__ ws) {
    const int t = threadIdx.x, l = t & 63, li = l & 15, g = l >> 4;
    const int w = __builtin_amdgcn_readfirstlane(t >> 6);
    const int bid = blockIdx.x, L = (bid & 7) * 32 + (bid >> 3);
    const int P = (L >> 7) * 8 + ((L >> 2) & 7), mem = ((L >> 5) & 3) * 4 + (L & 3);
    const bool act = mem < 14;
    const rsrc_t rws = mkrs(ws, WS_END);
    const v4u rws4 = mkrs4(ws, WS_END);
    const f4 z4 = {0.f, 0.f, 0.f, 0.f};
    const v4u sent4 = {SENT, SENT, SENT, SENT};
    STAMP(0); STAMPC(60);

    const rsrc_t rwm = mkrs(pwm, 512 * 3072 * 2);
    v2u e0[8]; v4u cv = {0, 0, 0, 0};
    if (w == 0) {
        cv = ld128(mkrs(pc, 1024), 16 * l);
#pragma unroll
        for (int i = 0; i < 8; ++i) e0[i] = ld64n(rwm, l * 6144 + 8 * L, ((i + L) & 7) * (64 * 6144));
    }
    const u32 tag = __builtin_amdgcn_raw_buffer_load_b32(rws, WS_EPOCH, 0, SC1) + 1u;
    SCHED();
    const float fl = (float)li, fg4 = (float)(4 * g), fw = (float)w;
    v4u aBR, aBI, aAI, aSR, aSI, FR, FI;
    f4 tc, ts;
    {
#pragma unroll
        for (int h = 0; h < 2; ++h)
#pragma unroll
            for (int jj = 0; jj < 2; ++jj) {
                const float n0 = fg4 + 16 * h + 2 * jj, a0 = n0 * fl * (1.f / 32), a1 = (n0 + 1.f) * fl * (1.f / 32);
                aBR[2 * h + jj] = pk(cosr(a0), cosr(a1));
                aBI[2 * h + jj] = li ? pk(-sinr(a0), -sinr(a1)) : pk(1.f, -1.f);
            }
        PIN(aBR); PIN(aBI);
    }
    SCHED();
    u32 cpf = 0;
    if (w == 7) {
        unsigned long long pcv; u32 len;
        asm volatile("s_getpc_b64 %0\n.Lpcref%=:\n\ts_mov_b32 %1, .Lcode_tail-.Lpcref%=" : "=s"(pcv), "=s"(len));
        cpf = __builtin_amdgcn_raw_buffer_load_b32(mkrs((const void*)pcv, len), l < 12 ? (l * 32 + (L & 31)) * 128 : 0x7FFFFFF0u, 0, 0);
    }
    const v4u xv = ld128n(mkrs(px, 2048 * 1024), 16 * l, row_of(L, w) * 1024);
    v4u wrr, wri;
    {
        const int tt = 2 * L + (w >> 2), j = w & 3;
        const int pos = 32 * (tt >> 6) + 2 * (tt & 15) + (j >> 1), kb = 2 * ((tt >> 4) & 3) + (j & 1);
        const int k = ((pos >> 2) & 15) + 16 * (4 * (pos >> 6) + (pos & 3)) + 256 * kb, km = (2048 - k) & 2047;
        const u32 o = l < 33 ? ((k * 514) & ~3) + 16 * l : l < 35 ? ((km * 514) & ~3) + (l - 33) * 512 : 0x7FFFFFF0u;
        wrr = ld128n(mkrs(pwr, 2048 * 514), o); wri = ld128n(mkrs(pwi, 2048 * 514), o);
    }
    st128c(sent4, rws, t * 16, WS_X1 + L * XSTR);
    const v4u rs1 = mkrs4(pw1, act ? 512 * 1792 * 2 : 0), rs3 = mkrs4(pw3, act ? 512 * 1792 * 2 : 0);
    const u32 wv1 = (4 * w + g) * 3584 + mem * 256 + (((li >> 1) ^ g) * 2 + (l & 1)) * 16, wv3 = wv1 ^ 128;
    const int rot = (L >> 2) & 7;
#define W13_DMA(first, nb, i) do { const int p_ = ((i) + rot) % (nb); \
        dma16(rs1, wv1, ((first) + p_) * 114688, L_BIG + p_ * 16384 + w * 1024); dma16(rs3, wv3, ((first) + p_) * 114688, L_BIG + p_ * 16384 + 8192 + w * 1024); } while (0)
    W13_DMA(0, NB1, 0);
    SCHED();
    STAMP(33);

    v4u GR, GI, HR, HI;
    f4 bc, bs, bc2, bs2;
    v2u A8;
    {
        fwd_pair(fg4 * fl * (1.f / 16), fl * (1.f / 16), FR, FI);
        const float sm = li < 8 ? fl * (1.f / 16) : (2.f * fl - 15.f) * (1.f / 32);
#pragma unroll
        for (int r = 0; r < 4; ++r) {
            aAI[r] = li < 8 ? FI[r] : FI[r] ^ 0x80008000u;
            const float a = (fg4 + r) * sm, c = cosr(a), s = li ? -sinr(a) : ((r & 1) ? -1.f : 1.f);
            aSR[r] = li < 8 ? pk(c, 0.f) : pk(0.f, c);
            aSI[r] = li < 8 ? pk(s, 0.f) : pk(0.f, s);
            const float a2 = (fg4 + r) * fl * (1.f / 512);
            tc[r] = cosr(a2); ts[r] = sinr(a2);
        }
        PIN(aAI); PIN(aSR); PIN(aSI); PIN(FR); PIN(FI); PIN(tc); PIN(ts);
    }
    SCHED();
    STAMP(1);

    if (w == 0) {
        f4 s0, s1;
#pragma unroll
        for (int i = 0; i < 2; ++i) {
            s0[2 * i] = bfr(silu(lo16(cv[i]))); s0[2 * i + 1] = bfr(silu(hi16(cv[i])));
            s1[2 * i] = bfr(silu(lo16(cv[2 + i]))); s1[2 * i + 1] = bfr(silu(hi16(cv[2 + i])));
        }
        lds<f4>(L_MOD1 + 32 * l) = s0; lds<f4>(L_MOD1 + 32 * l + 16) = s1;
        asm volatile("s_waitcnt lgkmcnt(0)" ::: "memory");
        float sc[8];
#pragma unroll
        for (int i = 0; i < 8; ++i) sc[i] = lds<float>(L_MOD1 + ((i + L) & 7) * 256 + 4 * l);
        f4 a = z4;
#pragma unroll
        for (int i = 0; i < 8; ++i) { a[0] += sc[i] * lo16(e0[i].x); a[1] += sc[i] * hi16(e0[i].x); a[2] += sc[i] * lo16(e0[i].y); a[3] += sc[i] * hi16(e0[i].y); }
        const float one = L >= 128 ? 1.f : 0.f;
#pragma unroll
        for (int i = 0; i < 4; ++i) a[i] = bfr(wave_sum(a[i])) + one;
        const v4u gr = {pk(a[0], a[1]), tag, pk(a[2], a[3]), tag};
        if (l == 0) st128c(gr, rws, WS_G0 + 16 * L);
    }
    SCHED();
    STAMP(2);

    f2 xh[4]; v2u xc0, xc1; float mean1;
    const u32 trd = (4 * (t & 1)) * 1040 + (t >> 1) * 4;
    {
        lds<v4u>(L_XS + w * 1024 + l * 16) = xv;
        asm volatile("" ::"v"(cpf));
        if (l < 35) { lds<v4u>(L_WR + w * 1120 + l * 16) = wrr; lds<v4u>(L_WR + w * 1120 + 560 + l * 16) = wri; }
        {
            const int sh = (L & 1) ? 0 : 16;
            const u32 fr = (wrr.x << sh) & 0xFFFF0000u, fi = (wri.x << sh) & 0xFFFF0000u;
#define RL(v, n) __builtin_bit_cast(float, __builtin_amdgcn_readlane((int)(v), n))
            const float h0r = RL(fr, 0) + RL(fr, 33), h0i = RL(fi, 0) - RL(fi, 33), h1r = RL(fr, 32) + RL(fr, 34), h1i = RL(fi, 32) - RL(fi, 34);
            if (l == 0) { lds<u32>(L_AB + w * 4) = pk(0.25f * (h0r + h1r), 0.25f * (h0i + h1i)); lds<u32>(L_AB + 32 + w * 4) = pk(0.25f * (h0r - h1r), 0.25f * (h0i - h1i)); }
        }
        BAR();
        const int kap = t >> 1, k1 = (kap >> 2) & 15, kk = 4 * (kap >> 6) + (kap & 3);
        const int k2 = k1 ? (kk < 8 ? k1 + 32 * kk : 512 - k1 - 32 * kk) : (kk < 8 ? 32 * kk : 32 * kk - 240);
        const u32 wo = L_WR + (t & 1) * 4480 + (L & 1) * 2 + k2 * 2;
        v4u o;
#pragma unroll
        for (int j = 0; j < 4; ++j) o[j] = (u32)lds<u16>(wo + j * 1120) | ((u32)lds<u16>(wo + j * 1120 + 560) << 16);
        if (w == 0) {
            const v4u ab = lds<v4u>(L_AB + (l & 3) * 16);
            if (l < 2) o = ab;
            if ((l & 62) == 2) st128c(ab, rws, WS_WT + L * XSTR + 8192 + (l & 1) * 16);
        }
        st128c(o, rws, t * 16, WS_WT + L * XSTR);
        st128c(sent4, rws, t * 16, WS_X2 + L * XSTR);
        SCHED();
        STAMP(3);
#pragma unroll
        for (int i = 0; i < NPA; ++i) W13_DMA(0, NB1, 1 + i);
        SCHED();
        f2 v[4];
#pragma unroll
        for (int i = 0; i < 4; ++i) v[i] = f2{lo16(xv[i]), hi16(xv[i])};
        const f2 s = (v[0] + v[1]) + (v[2] + v[3]);
        mean1 = wave_sum(s.x + s.y) * (1.f / 512);
        f2 q = {0.f, 0.f};
#pragma unroll
        for (int i = 0; i < 4; ++i) q += (v[i] - mean1) * (v[i] - mean1);
        const float rstd = __builtin_amdgcn_rsqf(wave_sum(q.x + q.y) * (1.f / 512) + 1e-5f);
        const v4u xa = ldtr2(L_XS + w * 1024 + 8 * l, L_XS + w * 1024 + 512 + 8 * l);
        xc0 = lds<v2u>(L_XS + w * 1024 + 64 * li + 8 * g); xc1 = lds<v2u>(L_XS + w * 1024 + 64 * li + 32 + 8 * g);
#pragma unroll
        for (int i = 0; i < 4; ++i) { xh[i] = (f2{lo16(xa[i]), hi16(xa[i])} - mean1) * rstd; PIN(xh[i]); }
    }
    SCHED();
    v4u m0[4];
#define G0_LOAD do { _Pragma("unroll") for (int i = 0; i < 4; ++i) m0[i] = ld128c(rws, l * 16 + i * 1024, WS_G0); } while (0)
    if (w == 0) { nap<D_E0>(); G0_LOAD; }
    SCHED();
    {
#pragma unroll
        for (int r = 0; r < 4; ++r) {
            const float a3 = (8.f * (fg4 + r) + fw) * fl * (1.f / 2048);
            bc[r] = cosr(a3); bs[r] = sinr(a3);
            const float a4 = (8.f * fl + fw) * (fg4 + r) * (1.f / 2048);
            bc2[r] = cosr(a4); bs2[r] = sinr(a4);
        }
        fwd_pair((8.f * fg4 + fw) * fl * (1.f / 128), fl * (1.f / 16), GR, GI);
        fwd_pair((8.f * fl + fw) * fg4 * (1.f / 128), (8.f * fl + fw) * (1.f / 128), HR, HI);
#pragma unroll
        for (int jb = 0; jb < 2; ++jb) {
            const float a = (float)((2 * g + jb) * (li >> 1)) * 0.125f, c = cosr(a), s = sinr(a);
            A8[jb] = (li & 1) ? pk(-s, c) : pk(c, s);
        }
        PIN(GR); PIN(GI); PIN(HR); PIN(HI); PIN(bc); PIN(bs); PIN(bc2); PIN(bs2); PIN(A8);
    }
    SCHED();
    STAMP(32);
    if (w == 0) {
        RETRY((m0[0].y != tag) | (m0[0].w != tag) | (m0[1].y != tag) | (m0[1].w != tag) | (m0[2].y != tag) | (m0[2].w != tag) | (m0[3].y != tag) | (m0[3].w != tag), G0_LOAD);
#pragma unroll
        for (int i = 0; i < 4; ++i) lds<v2u>(L_MOD0 + i * 512 + l * 8) = v2u{m0[i].x, m0[i].z};
    }
    waitvm<2 * NPA>();
    BAR();
    STAMP(4);

    v4u e1[8];
    {
        const v4u sa = ldtr2(L_MOD0 + 8 * l, L_MOD0 + 512 + 8 * l), ca = ldtr2(L_MOD0 + 1024 + 8 * l, L_MOD0 + 1536 + 8 * l);
        if (w == 7 && l == 0) {
            __builtin_amdgcn_raw_buffer_store_b32(tag, rws, WS_TK + 4 * L, 0, SC1);
            if (L == 0) __builtin_amdgcn_raw_buffer_store_b32(tag, rws, WS_EPOCH, 0, SC1);
        }
        if (NPB > 0) W13_DMA(0, NB1, 1 + NPA);
        v4u ha;
#pragma unroll
        for (int i = 0; i < 4; ++i) { const f2 h = xh[i] * f2{lo16(ca[i]), hi16(ca[i])} + f2{lo16(sa[i]), hi16(sa[i])}; ha[i] = pk(h.x, h.y); }
        const f4 yr = mfma16(ha, aBR, z4), yi = mfma16(ha, aBI, z4);
        if (NPB > 1) W13_DMA(0, NB1, 2 + NPA);
        const v4u yb = twid_pack(yr, yi, tc, ts);
        const f4 xr = mfma16(FR, yb, z4), xi = mfma16(aAI, yb, z4), sr = mfma16(aSR, yb, z4), si = mfma16(aSI, yb, z4);
        if (NPB > 2) W13_DMA(0, NB1, 3 + NPA);
        const v4u o1 = pk4(xr, xi), o2 = pk4(sr, si);
        v4u o = li ? o1 : o2;
        lds<v4u>(L_T1 + w * 1040 + l * 16) = o;
        BAR();
#pragma unroll
        for (int j = 0; j < 4; ++j) o[j] = lds<u32>(L_T1 + trd + j * 1040);
        st128c(o, rws, t * 16, WS_X1 + L * XSTR);
    }
    SCHED();
    STAMP(5);
    __builtin_amdgcn_s_barrier();
    u32 tk = __builtin_amdgcn_raw_buffer_load_b32(rws, (t >> 1) * 4, WS_TK, SC1);
#pragma unroll
    for (int i = 0; i < NPC; ++i) W13_DMA(0, NB1, 1 + NPA + NPB + i);
    if (!act) {
#pragma unroll
        for (int i = 0; i < 8; ++i) e1[i] = ld128n(rwm, (l >> 3) * 6144 + (l & 7) * 16, (64 * w + 8 * i) * 6144 + 2048 + (2 * P + mem - 14) * 128);
        f2 a[4] = {{0.f, 0.f}, {0.f, 0.f}, {0.f, 0.f}, {0.f, 0.f}};
#pragma unroll
        for (int i = 0; i < 8; ++i) {
            const float sc = lds<float>(L_MOD1 + (64 * w + 8 * i + (l >> 3)) * 4);
#pragma unroll
            for (int j = 0; j < 4; ++j) a[j] += sc * f2{lo16(e1[i][j]), hi16(e1[i][j])};
        }
#pragma unroll
        for (int j = 0; j < 4; ++j) { a[j].x = dppadd<0x128, 0xF>(a[j].x); a[j].y = dppadd<0x128, 0xF>(a[j].y); }
        if (!(l & 8)) { lds<f4>(L_E2 + (4 * w + g) * 256 + (l & 7) * 32) = f4{a[0].x, a[0].y, a[1].x, a[1].y}; lds<f4>(L_E2 + (4 * w + g) * 256 + (l & 7) * 32 + 16) = f4{a[2].x, a[2].y, a[3].x, a[3].y}; }
    }
    RETRY(tk != tag, tk = __builtin_amdgcn_raw_buffer_load_b32(rws, (t >> 1) * 4, WS_TK, SC1));
    VM0();
    BAR();
    STAMP(6);
    nap<D_E1>();
    const u32 xrd = (t >> 1) * XSTR + L * 32 + (t & 1) * 16;
    v4u zin, wf4, wb4;
    ld128a(zin, rws4, WS_X1 + xrd);
    ld128a(wf4, rws4, WS_WT + xrd);
    ld128a(wb4, rws4, L == 0 ? WS_WT + xrd + 8192 : 0x7FFFFFF0u);
    const u32 h2_w = (P * 128 + mem * 8 + w) * 1024 + (4 * li + g) * 16;
    st128c(sent4, rws, h2_w, WS_H2);
    SCHED();
    v4u wf[32];
    u32 wtr = L_BIG + (g & 1) * 8192 + (4 * (l >> 5) + ((l >> 2) & 3)) * 256 + ((w ^ ((l >> 2) & 3) ^ ((g & 1) * 4)) * 32) + (((l & 1) << 1) | ((l >> 1) & 1)) * 8;
    PIN(wtr);
#define W13_READ(first, nb) do { u32 hi_ = wtr + 65536; PIN(hi_); _Pragma("unroll") for (int p = 0; p < (nb); ++p) { \
        const u32 o_ = (p < 4 ? wtr : hi_) + (p & 3) * 16384; \
        wf[2 * ((first) + p)] = ldtr2(o_, o_ + 4096); wf[2 * ((first) + p) + 1] = ldtr2(o_ + 2048, o_ + 6144); } } while (0)
    W13_READ(0, NB1);
    if (!act && w == 0) {
        float a = 0.f;
#pragma unroll
        for (int i = 0; i < 32; ++i) a += lds<float>(L_E2 + i * 256 + l * 4);
        const int si = 2 * P + mem - 14;
        a = bfr(a) + ((si >> 3) == 2 ? 1.f : 0.f);
        const float b = __builtin_bit_cast(float, __builtin_amdgcn_update_dpp(0, __builtin_bit_cast(int, a), 0xB1, 0xF, 0xF, true));
        if (!(l & 1)) __builtin_amdgcn_raw_buffer_store_b64(v2u{pk(a, b), tag}, rws, WS_G1 + si * 256 + l * 4, 0, SC1);
        VM0();
    }
    BAR();
#pragma unroll
    for (int i = 0; i < NPD; ++i) W13_DMA(NB1, NB2, i);
    waitv<2 * NPD + 3>(zin);
    RETRY(vmax(zin, 0) == SENT, zin = ld128c(rws, WS_X1 + xrd));
    STAMP(7);

    {
        f4 re = mfma16(zin, FR, z4), im = mfma16(zin, FI, z4);
        v4u zb = twid_pack(re, im, bc, bs);
        re = mfma16(GR, zb, z4); im = mfma16(GI, zb, z4);
        lds<v4u>(L_E1 + w * 1088 + l * 16) = pk4(re, im);
        BAR();
        const u32 eo = 2 * g * 1088 + (32 * w + 2 * li) * 4;
        const v2u u0 = lds<v2u>(L_E1 + eo), u1 = lds<v2u>(L_E1 + eo + 1088);
        f4 d[2] = {mfma16k(A8, v2u{u0.x, u1.x}, z4), mfma16k(A8, v2u{u0.y, u1.y}, z4)};
        waitv<2 * NPD>(wf4); waitv<2 * NPD>(wb4);
        f4 f[2];
#pragma unroll
        for (int m = 0; m < 2; ++m)
#pragma unroll
            for (int b = 0; b < 2; ++b) {
                const float wr = lo16(wf4[2 * m + b]), wi = hi16(wf4[2 * m + b]), zr = d[m][2 * b], zi = d[m][2 * b + 1];
                f[m][2 * b] = zr * wr - zi * wi; f[m][2 * b + 1] = zr * wi + zi * wr;
            }
        if (L == 0) {
            v2u A1, A0;
#pragma unroll
            for (int jb = 0; jb < 2; ++jb) {
                const float a = (float)((2 * g + jb) * ((li >> 1) + 1)) * 0.125f, c = cosr(a), s = sinr(a);
                A1[jb] = (li & 1) ? pk(-s, -c) : pk(c, -s);
                A0[jb] = A8[jb] ^ 0x80000000u;
            }
#pragma unroll
            for (int m = 0; m < 2; ++m) {
                const int pos = 32 * w + 2 * li + m, q = ((pos >> 2) & 15) + 16 * (4 * (pos >> 6) + (pos & 3)), qm = (256 - q) & 255;
                const int pm = 64 * (qm >> 6) + 4 * (qm & 15) + ((qm >> 4) & 3);
                const v2u um = {lds<u32>(L_E1 + 2 * g * 1088 + pm * 4), lds<u32>(L_E1 + (2 * g + 1) * 1088 + pm * 4)};
                const f4 m1 = mfma16k(A1, um, z4), mz = mfma16k(A0, um, z4);
#pragma unroll
                for (int b = 0; b < 2; ++b) {
                    const float wr = lo16(wb4[2 * m + b]), wi = hi16(wb4[2 * m + b]);
                    const float zr = q ? m1[2 * b] : mz[2 * b], zi = q ? m1[2 * b + 1] : mz[2 * b + 1];
                    f[m][2 * b] += zr * wr - zi * wi; f[m][2 * b + 1] += zr * wi + zi * wr;
                }
            }
        }
        d[0] = mfma16k(A8, v2u{pk(f[0][1], f[0][0]), pk(f[0][3], f[0][2])}, z4);
        d[1] = mfma16k(A8, v2u{pk(f[1][1], f[1][0]), pk(f[1][3], f[1][2])}, z4);
        lds<v2u>(L_E2 + eo) = v2u{pk(d[0][0], d[0][1]), pk(d[1][0], d[1][1])};
        lds<v2u>(L_E2 + eo + 1088) = v2u{pk(d[0][2], d[0][3]), pk(d[1][2], d[1][3])};
        BAR();
        zb = lds<v4u>(L_E2 + w * 1088 + l * 16);
        re = mfma16(zb, HR, z4); im = mfma16(zb, HI, z4);
        zb = twid_pack(re, im, bc2, bs2);
        re = mfma16(FR, zb, z4); im = mfma16(FI, zb, z4);
        st128c(pk4(im, re), rws, t * 16, WS_X2 + L * XSTR);
    }
    SCHED();
    STAMP(8);
    v4u cBR, cBI, cSR, cSI, cB0, cB1;
    {
        v4u m = ld128c(rws, t * 16, WS_G1);
        SCHED();
        const float sg = g < 2 ? 1.f : -1.f;
#pragma unroll
        for (int r = 0; r < 4; ++r) {
            const float kk = fg4 + r, a = kk * fl * (1.f / 16), c = cosr(a), s = sinr(a);
            cBR[r] = pk(c, -sg * s); cBI[r] = pk(s, sg * c);
            const float a2 = (2.f * kk - 15.f) * fl * (1.f / 32), p1 = (2.f * fl + 1.f) * kk * (1.f / 32);
            u32 v0 = pk(2.f * c, -2.f * s), v1 = pk(2.f * cosr(p1), -2.f * sinr(p1)), vs = v0;
            if (r == 0) { vs = g == 0 ? pk(1.f, (li & 1) ? -1.f : 1.f) : vs; v0 = g == 0 ? pk(1.f, 1.f) : v0; v1 = g == 0 ? pk(1.f, -1.f) : v1; }
            cSR[r] = g < 2 ? vs : 0u; cSI[r] = g < 2 ? 0u : pk(2.f * cosr(a2), -2.f * sinr(a2));
            cB0[r] = v0; cB1[r] = v1;
        }
        PIN(cBR); PIN(cBI); PIN(cSR); PIN(cSI); PIN(cB0); PIN(cB1);
        SCHED();
        RETRY((m.y != tag) | (m.w != tag), m = ld128c(rws, t * 16, WS_G1));
        lds<v2u>(L_MOD1 + t * 8) = v2u{m.x, m.z};
    }
    VM0();
    BAR();
    STAMP(9);
    nap<D_E2>();
    ld128a(zin, rws4, WS_X2 + xrd);
    SCHED();
    W13_READ(NB1, NB2);
    f2 ga[4], sb[4], cb[4], xp[4];
#pragma unroll
    for (int h = 0; h < 2; ++h) {
        const u32 o = L_MOD1 + 64 * li + 32 * h + 8 * g;
        const v2u a = lds<v2u>(o), b = lds<v2u>(o + 1024), c = lds<v2u>(o + 2048), x = h ? xc1 : xc0;
#pragma unroll
        for (int i = 0; i < 2; ++i) {
            ga[2 * h + i] = f2{lo16(a[i]), hi16(a[i])} * (1.f / 1048576); sb[2 * h + i] = f2{lo16(b[i]), hi16(b[i])};
            cb[2 * h + i] = f2{lo16(c[i]), hi16(c[i])}; xp[2 * h + i] = f2{lo16(x[i]), hi16(x[i])} - mean1;
            PIN(ga[2 * h + i]); PIN(sb[2 * h + i]); PIN(cb[2 * h + i]); PIN(xp[2 * h + i]);
        }
    }
    BAR();
#pragma unroll
    for (int i = 0; i < NPG; ++i) W13_DMA(NB1 + NB2, NB3, i);
    waitv<2 * NPG>(zin);
    RETRY(vmax(zin, 0) == SENT, zin = ld128c(rws, WS_X2 + xrd));
    STAMP(10);

    v4u xo;
    {
#pragma unroll
        for (int j = 0; j < 4; ++j) lds<u32>(L_T0 + trd + j * 1040) = zin[j];
        BAR();
        const v4u va = lds<v4u>(L_T0 + w * 1040 + l * 16);
        const f4 sr = mfma16(va, cSR, z4), si = mfma16(va, cSI, z4);
        f4 ur = mfma16(va, cBR, z4), ui = mfma16(va, cBI, z4);
        if (g == 0) { ur[0] = sr[0]; ui[0] = si[0]; }
        f4 tr = ur * tc - ui * ts, ti = ui * tc + ur * ts;
        PIN(tr); PIN(ti);
        const v4u ta = pk4(tr, ti);
        const f4 y0 = mfma16(ta, cB0, z4), y1 = mfma16(ta, cB1, z4);
        f2 v[4] = {xp[0] + ga[0] * f2{y0[0], y0[1]}, xp[1] + ga[1] * f2{y0[2], y0[3]}, xp[2] + ga[2] * f2{y1[0], y1[1]}, xp[3] + ga[3] * f2{y1[2], y1[3]}};
        const f2 s1 = (v[0] + v[1]) + (v[2] + v[3]), s2 = (v[0] * v[0] + v[1] * v[1]) + (v[2] * v[2] + v[3] * v[3]);
        const float m2 = wave_sum(s1.x + s1.y) * (1.f / 512);
        const float rstd = __builtin_amdgcn_rsqf(fmaxf(wave_sum(s2.x + s2.y) * (1.f / 512) - m2 * m2, 0.f) + 1e-5f);
        v4u ho;
#pragma unroll
        for (int i = 0; i < 4; ++i) { const f2 h = (v[i] - m2) * (cb[i] * rstd) + sb[i]; ho[i] = pk(h.x, h.y); }
        st128c(ho, rws, h2_w, WS_H2);
        SCHED();
#pragma unroll
        for (int i = 0; i < 4; ++i) { const f2 x = v[i] + mean1; xo[i] = pk(x.x, x.y); }
    }
    STAMP(11);
    VM0();
    BAR();

    const v4u rs2 = mkrs4(pw2, 1792 * 512 * 2);
    const int P2 = L >> 4, rp = (L >> 3) & 1, ct = L & 7;
#define W2_DECL const u32 v2a = (l >> 3) * 1024 + ct * 128 + ((((l >> 1) & 3) ^ ((l >> 4) & 1)) * 2 + (l & 1)) * 16, v2b = v2a ^ 64; \
        v4u w2f[28], hb[3][7]; u32 w2a[4], fl16
#define W2_ADDR() do { const int l_ = fresh_lane(); _Pragma("unroll") for (int nt = 0; nt < 4; ++nt) { \
        w2a[nt] = L_BIG + w * 12288 + (8 * (l_ >> 4) + ((l_ >> 2) & 3)) * 128 + (l_ & 3) * 8 + ((nt ^ ((l_ >> 3) & 3)) * 32); PIN(w2a[nt]); } } while (0)
#define W2_FRAG(slot, nt) ldtr2(w2a[nt] + (slot) * 4096, w2a[nt] + (slot) * 4096 + 512)
#define W2_DMA(ks, i, slot) dma16(rs2, ((i) & 1) ? v2b : v2a, (w * 224 + (ks) * 32 + (i) * 8) * 1024, L_BIG + w * 12288 + (slot) * 4096 + (i) * 1024)
#define FL_LOAD fl16 = __builtin_amdgcn_raw_buffer_load_b32(rws, (fresh_lane() & 15) * 4, WS_FL + P2 * 128, SC1)
#define H_LOAD(q) do { _Pragma("unroll") for (int k = 0; k < 7; ++k) hb[(q) % 3][k] = ld128c(rws, h_rd + (k & 3) * 1024, (q) * 114688 + (k >> 2) * 4096); } while (0)

    auto gemm2 = [&](const v4u (&w2f)[28], v4u (&hb)[3][7], u32 h_rd, bool slow) __attribute__((always_inline)) {
        STAMP(13); STAMPC(62);
        const int l = fresh_lane(), li = l & 15, g = l >> 4;
        const int qq = w >> 1, cg = (w & 1) * 4 + g, pr = (2 * qq + rp) * 16 + li;
        const u32 xro = WS_XR + (P2 * 128 + pr) * 1024 + (4 * (2 * ct + (cg >> 2)) + 2 * (cg & 1)) * 16 + ((cg >> 1) & 1) * 8;
        v2u xa, xb;
#pragma unroll
        for (int q = 0; q < 4; ++q) {
            if (q > 0) RETRY(vmaxn<7>(hb[q % 3]) == SENT, { if (slow) nap<8>(); H_LOAD(q); });
            STAMP(34 + q);
            f4 o[4] = {z4, z4, z4, z4};
#pragma unroll
            for (int k = 0; k < 7; ++k)
#pragma unroll
                for (int nt = 0; nt < 4; ++nt) o[nt] = mfma16(w2f[4 * k + nt], hb[q % 3][k], o[nt]);
            if (q == 0) H_LOAD(3);
            if (q == 1) { xa = ld64c(rws, xro); xb = ld64c(rws, xro + 16); }
#pragma unroll
            for (int nt = 0; nt < 4; ++nt) lds<f4>((q < 3 ? L_BIG + w * 12288 + q * 4096 : w * 4096) + nt * 1024 + l * 16) = o[nt];
            SCHED();
        }
        STAMP(14);
        const v4u gbv = lds<v4u>(L_MOD1 + 3072 + ct * 128 + cg * 16);
        const u32 po = (qq < 3 ? L_BIG + qq * 4096 : 0) + (cg >> 1) * 1024 + (cg & 1) * 512 + li * 16, ps = qq < 3 ? 12288 : 4096;
        const int m2 = pr >> 3, L2 = (P2 >> 3) * 128 + (m2 >> 2) * 32 + (P2 & 7) * 4 + (m2 & 3);
        u32 oo = row_of(L2, pr & 7) * 1024 + ct * 128 + cg * 16;
        PIN(oo);
        BAR();
        f4 s0 = z4, s1 = z4;
#pragma unroll
        for (int i = 0; i < 8; ++i) { s0 += lds<f4>(po + i * ps); s1 += lds<f4>(po + i * ps + 256); }
        const v4u o = {pk(lo16(xa.x) + lo16(gbv.x) * s0[0], hi16(xa.x) + hi16(gbv.x) * s0[1]), pk(lo16(xa.y) + lo16(gbv.y) * s0[2], hi16(xa.y) + hi16(gbv.y) * s0[3]),
                       pk(lo16(xb.x) + lo16(gbv.z) * s1[0], hi16(xb.x) + hi16(gbv.z) * s1[1]), pk(lo16(xb.y) + lo16(gbv.w) * s1[2], hi16(xb.y) + hi16(gbv.w) * s1[3])};
        __builtin_amdgcn_raw_buffer_store_b128(o, mkrs(pout, 2048 * 1024), oo, 0, 0);
        STAMP(15); STAMPC(63);
        STAMP_FLUSH();
    };

    int mem_ = mem; asm volatile("" : "+s"(mem_));
    if (act) {
#define MT(n) ((n) < 4 ? (n) >> 1 : 2 + ((n) & 1))
#define KH(n) ((n) < 4 ? (n) & 1 : ((n) >> 1) & 1)
#define A_OFF(n) (WS_H2 + MT(n) * 32768 + KH(n) * 512)
#define A_LOAD(n) do { sa0[(n) & 1] = ld128c(rws, a_rd, A_OFF(n)); sa1[(n) & 1] = ld128c(rws, a_rd, A_OFF(n) + 16384); } while (0)
#define A_PUT(n) do { RETRY(vmax(sa1[(n) & 1], vmax(sa0[(n) & 1], 0)) == SENT, A_LOAD(n)); \
        lds<v4u>(a_wr + ((n) & 1) * SLOT) = sa0[(n) & 1]; lds<v4u>(a_wr + ((n) & 1) * SLOT + 256) = sa1[(n) & 1]; } while (0)
        const int l = fresh_lane();
        const u32 a_rd = (P * 128 + 2 * w + (l >> 5)) * 1024 + (l & 31) * 16, a_wr = (l & 31) * 528 + (2 * w + (l >> 5)) * 16;
        v4u sa0[2], sa1[2];
        nap<D_E3>();
        A_LOAD(0); A_LOAD(1);
        SCHED();
        W13_READ(NB1 + NB2, NB3);
        W2_DECL;
        const int ft = 8 * mem + w;
        const u32 hh_w = WS_HH + (P * 8 + ((l >> 4) & 1)) * 57344 + (ft >> 1) * 1024 + ((ft & 1) * 2 + (l >> 5)) * 256 + (l & 15) * 16;
        const u32 b_rd = (l >> 5) * 528 + (l & 31) * 16;
        u32 h_rd = 0;
        A_PUT(0);
        A_LOAD(2);
        BAR();
        STAMP(12); STAMPC(61);
        constexpr int NBF = 6;
        v4u bf[NBF];
        f16v acc[2];
#define B_READ(T) bf[(T) % NBF] = lds<v4u>(b_rd + (((T) >> 4) & 1) * SLOT + ((T) & 15) * 1056)
#define HH_STORE(mt) st128c(v4u{pk(hv[0], hv[1]), pk(hv[2], hv[3]), pk(hv[4], hv[5]), pk(hv[6], hv[7])}, rws, hh_w, (mt) * 114688)
#pragma unroll
        for (int T = 0; T < NBF; ++T) B_READ(T);
#pragma unroll
        for (int n = 0; n < 8; ++n) {
            const int ai = MT(n) & 1;
            float hv[8];
#pragma unroll
            for (int u = 0; u < 16; ++u) {
                const int T = 16 * n + u;
                f16v cin = acc[ai];
                if (KH(n) == 0 && u == 0) cin = f16v{0.f, 0.f, 0.f, 0.f, 0.f, 0.f, 0.f, 0.f, 0.f, 0.f, 0.f, 0.f, 0.f, 0.f, 0.f, 0.f};
                acc[ai] = mfma32(wf[16 * KH(n) + u], bf[T % NBF], cin);
                if (u + NBF < 16) B_READ(T + NBF);
                if (u == 17 - NBF) {
                    if (n == 3) waitvm<2>();
                    STAMP(16 + 2 * n);
                    BAR();
                    STAMP(17 + 2 * n);
                    if (n < 7) { B_READ(T + NBF - 1); B_READ(T + NBF); }
                    if (n == 3 && w == 0 && l == 0) __builtin_amdgcn_raw_buffer_store_b32(tag, rws, WS_FL + P * 128 + mem * 4, 0, SC1);
                }
                if (u > 17 - NBF && n < 7) B_READ(T + NBF);
                if (u == 1 && n < 7) A_PUT(n + 1);
                if (n == 0 && u == 2) st128c(xo, rws, h2_w, WS_XR);
                if (n == 1 && u >= 12 && u < 15) st128c(sent4, rws, hh_w, (u - 11) * 114688);
                if (n < 2 && u >= 2 && u < 8) W2_DMA((6 * n + u - 2) >> 2, (6 * n + u - 2) & 3, (6 * n + u - 2) >> 2);
                if (n == 3 && u == 2) W2_ADDR();
                if (n == 3 && u >= 2 && u < 6) w2f[u - 2] = W2_FRAG(0, u - 2);
                if (n == 4 && u >= 2 && u < 6) W2_DMA(3, u - 2, 0);
                if (u == 8 && n < 5) A_LOAD(n + 3);
                if (n == 5 && u == 8) FL_LOAD;
                if (n == 6 && u == 2) { VM0(); W2_ADDR(); }
                if (n == 6 && u >= 2 && u < 8) { w2f[2 * u] = W2_FRAG(((2 * u) >> 2) % 3, (2 * u) & 3); w2f[2 * u + 1] = W2_FRAG(((2 * u) >> 2) % 3, (2 * u + 1) & 3); }
                if (n == 6 && u >= 12) { W2_DMA(4, u - 12, 1); W2_DMA(5, u - 12, 2); W2_DMA(6, u - 12, 0); }
                if (n == 2 || n == 4 || n == 7) {
                    if (u >= 2 && u < 10) hv[u - 2] = silu(acc[ai ^ 1][u - 2]) * acc[ai ^ 1][u + 6];
                    if (u == 10) HH_STORE(n == 7 ? 2 : (n >> 1) - 1);
                }
                SCHED();
            }
        }
        {
            float hv[8];
#pragma unroll
            for (int r = 0; r < 8; ++r) hv[r] = silu(acc[1][r]) * acc[1][r + 8];
            HH_STORE(3);
        }
        SCHED();
        __builtin_amdgcn_s_barrier();
        RETRY(fl16 != tag, FL_LOAD);
        h_rd = WS_HH + (P2 * 8 + rp) * 57344 + w * 7168 + fresh_lane() * 16;
        H_LOAD(0); H_LOAD(1); H_LOAD(2);
        SCHED();
        waitvm<23>();
#pragma unroll
        for (int u = 16; u < 28; ++u) w2f[u] = W2_FRAG((u >> 2) % 3, u & 3);
        gemm2(w2f, hb, h_rd, false);
    }
    if (mem_ >= 14) {
        const int l = fresh_lane();
        W2_DECL;
        st128c(xo, rws, h2_w, WS_XR);
        W2_ADDR();
#pragma unroll
        for (int r = 0; r < 3; ++r) {
#pragma unroll
            for (int u = 0; u < 12; ++u) if (12 * r + u < 28) W2_DMA(3 * r + (u >> 2), u & 3, u >> 2);
            VM0();
            if (r == 0) { BAR(); if (w == 0 && l == 0) __builtin_amdgcn_raw_buffer_store_b32(tag, rws, WS_FL + P * 128 + mem * 4, 0, SC1); }
#pragma unroll
            for (int u = 0; u < 12; ++u) if (12 * r + u < 28) w2f[12 * r + u] = W2_FRAG(u >> 2, u & 3);
            asm volatile("s_waitcnt lgkmcnt(0)" ::: "memory");
        }
        nap<64>();
        FL_LOAD;
        RETRY(fl16 != tag, { nap<8>(); FL_LOAD; });
        nap<48>();
        const u32 h_rd = WS_HH + (P2 * 8 + rp) * 57344 + w * 7168 + l * 16;
        H_LOAD(0); H_LOAD(1); H_LOAD(2);
        gemm2(w2f, hb, h_rd, true);
        asm volatile(".Lcode_tail:");
    }
}

extern "C" void kernel_launch(void* const* d_in, const int* in_sizes, int n_in, void* d_out, int out_size, void* d_ws, size_t ws_size,
                              hipStream_t stream) {
    (void)in_sizes; (void)n_in; (void)out_size; (void)ws_size;
    fused_kernel<<<256, 512, 0, stream>>>((const u16*)d_in[0], (const u16*)d_in[1], (const u16*)d_in[2], (const u16*)d_in[4],
                                          (const u16*)d_in[5], (const u16*)d_in[6], (const u16*)d_in[7], (const u16*)d_in[8],
                                          (u16*)d_out, (unsigned char*)d_ws);
}
```

```cpp
#include <hip/hip_runtime.h>
#include <hip/hip_bf16.h>
#include <type_traits>

typedef unsigned u32;
typedef unsigned short u16;
typedef u32 v4u __attribute__((ext_vector_type(4)));
typedef u32 v2u __attribute__((ext_vector_type(2)));
typedef float f2 __attribute__((ext_vector_type(2)));
typedef float f4 __attribute__((ext_vector_type(4)));
typedef float f16v __attribute__((ext_vector_type(16)));
typedef short s4 __attribute__((ext_vector_type(4)));
typedef __bf16 b8 __attribute__((ext_vector_type(8)));
typedef __bf16 b2 __attribute__((ext_vector_type(2)));
typedef __amdgpu_buffer_rsrc_t rsrc_t;
#define LAS __attribute__((address_space(3)))
#define DEV __device__ __forceinline__

constexpr u32 WS_EPOCH = 0;
constexpr u32 WS_TK = 16384;
constexpr u32 WS_TKB = 24576;
constexpr u32 WS_FL = 32768;
constexpr u32 WS_G0 = 4096;
constexpr u32 WS_G1 = 8192;
constexpr u32 XSTR = 8320;
constexpr u32 WS_X1 = 65536;
constexpr u32 WS_WT = WS_X1 + 256 * XSTR;
constexpr u32 WS_X2 = WS_WT + 256 * XSTR;
constexpr u32 WS_H2 = WS_X2 + 256 * XSTR;
constexpr u32 WS_XR = WS_H2 + 2048 * 1024;
constexpr u32 WS_HH = WS_XR + 2048 * 1024;
constexpr u32 WS_END = WS_HH + 2048 * 1792 * 2;
constexpr u32 WS_DBG = WS_END;

constexpr u32 SLOT = 16384;
constexpr u32 L_PARK = 65536;
constexpr u32 L_XS = 0;
constexpr u32 L_T0 = 8192;
constexpr u32 L_T1 = L_T0 + 8320;
constexpr u32 WRS = 2064;
#define WRB(v) ((v) * WRS + ((v) >> 2) * 32)
constexpr u32 L_WR = L_T0;
constexpr u32 L_MF = L_XS;
constexpr u32 L_E1 = L_XS;
constexpr u32 L_E2 = L_T1 + 8320;
constexpr u32 L_MOD0 = 33792;
constexpr u32 L_MOD1 = L_MOD0 + 2048;
constexpr u32 L_TAB = 40960;
constexpr u32 L_SCRAP = L_TAB + 16384;
constexpr u32 L_BIG = 65536;
constexpr u32 L_TOTAL = 163840;
static_assert(L_E2 + 8 * 1088 <= L_MOD0 && L_MOD1 + 4096 <= L_BIG && L_TOTAL <= 163840, "lds");

constexpr u32 SENT = 0xFFFFFFFFu;
constexpr u32 SPIN_CAP = 1u << 16;
constexpr int SC1 = 16;
#define NT 0
#define D_E0 3
#define D_E1 0
#define D_E2 0
#define D_IDLE 64
#define D_E3 0
#define NB1 6
#define NB2 5
#define NB3 5
static_assert(NB1 + NB2 + NB3 == 16 && NB1 <= 6 && NB2 <= 6 && NB3 <= 6 && NB1 >= 4, "schedule");
#define F2P 1
#define NSB 8
#define NSC 6
#define TOUCH 0
constexpr int GW = 1;
#define N1 4
#define NA 4
#define ND 2
#define NG 4
#define D_TK 0
constexpr u32 OOB = 0x7FFF0000u;

constexpr double c_sin(int num, int den) {
    num = ((num % den) + den) % den;
    double sgn = 1.0;
    if (2 * num >= den) { num -= den / 2; sgn = -1.0; }
    if (4 * num > den) num = den / 2 - num;
    const double x = 6.283185307179586476925 * num / den;
    double term = x, sum = x;
    for (int i = 1; i < 14; ++i) { term *= -x * x / ((2 * i) * (2 * i + 1)); sum += term; }
    return sgn * sum;
}
constexpr double c_cos(int num, int den) { return c_sin(4 * num + den, 4 * den); }
constexpr u32 c_f32(double v) { return __builtin_bit_cast(u32, (float)v); }
constexpr u32 c_bf(double v) { const u32 u = c_f32(v + 0.0); return ((u + 0x7FFFu + ((u >> 16) & 1u)) >> 16) & 0xFFFFu; }
constexpr u32 c_pk(double a, double b) { return c_bf(a) | (c_bf(b) << 16); }
enum { T_aBR, T_aBI, T_FR, T_aAI, T_aSR, T_aSI, T_tc, T_ts, T_FI, T_A8, T_cBR, T_cBI, T_cSR, T_cSI, T_cB0, T_cB1, T_N };
struct Tab { u32 v[T_N * 256]; };
constexpr Tab make_tab() {
    Tab T{};
    for (int l = 0; l < 64; ++l)
        for (int r = 0; r < 4; ++r) {
            const int li = l & 15, g = l >> 4, n = 4 * g + r, o = l * 4 + r;
            const int n0 = 4 * g + 16 * (r >> 1) + 2 * (r & 1);
            T.v[T_aBR * 256 + o] = c_pk(c_cos(n0 * li, 32), c_cos((n0 + 1) * li, 32));
            T.v[T_aBI * 256 + o] = li ? c_pk(-c_sin(n0 * li, 32), -c_sin((n0 + 1) * li, 32)) : c_pk(1, -1);
            const double c = c_cos(n * li, 16), s = c_sin(n * li, 16);
            T.v[T_FR * 256 + o] = c_pk(c, s);
            T.v[T_FI * 256 + o] = c_pk(-s, c);
            T.v[T_aAI * 256 + o] = li < 8 ? c_pk(-s, c) : c_pk(s, -c);
            T.v[T_aSR * 256 + o] = li < 8 ? c_pk(c, 0) : c_pk(0, c_cos(n * (2 * li - 15), 32));
            T.v[T_aSI * 256 + o] = li == 0 ? c_pk((r & 1) ? -1 : 1, 0) : li < 8 ? c_pk(-s, 0) : c_pk(0, -c_sin(n * (2 * li - 15), 32));
            T.v[T_tc * 256 + o] = c_f32(c_cos(n * li, 512));
            T.v[T_ts * 256 + o] = c_f32(c_sin(n * li, 512));
            const int b = 2 * g + (r & 1), a8 = b * ((li >> 1) + (r >> 1)) + 2 * (li & 1);
            T.v[T_A8 * 256 + o] = c_pk(c_cos(a8, 8), (r >> 1) ? -c_sin(a8, 8) : c_sin(a8, 8));
            const double sg = g < 2 ? 1 : -1;
            T.v[T_cBR * 256 + o] = c_pk(c, -sg * s);
            T.v[T_cBI * 256 + o] = c_pk(s, sg * c);
            T.v[T_cSR * 256 + o] = n == 0 ? c_pk(1, (li & 1) ? -1 : 1) : g < 2 ? c_pk(2 * c, -2 * s) : 0u;
            T.v[T_cSI * 256 + o] = g < 2 ? 0u : c_pk(2 * c_cos((2 * n - 15) * li, 32), -2 * c_sin((2 * n - 15) * li, 32));
            const double k = 1.0 / 1048576;
            T.v[T_cB0 * 256 + o] = n == 0 ? c_pk(k, k) : c_pk(2 * k * c, -2 * k * s);
            T.v[T_cB1 * 256 + o] = n == 0 ? c_pk(k, -k) : c_pk(2 * k * c_cos((2 * li + 1) * n, 32), -2 * k * c_sin((2 * li + 1) * n, 32));
        }
    return T;
}
__device__ __attribute__((aligned(128))) const Tab g_tab = make_tab();

__shared__ __attribute__((aligned(16))) unsigned char smem[L_TOTAL];

DEV u32 pk(float lo, float hi) { f2 v = {lo, hi}; return __builtin_bit_cast(u32, __builtin_convertvector(v, b2)); }
DEV float lo16(u32 x) { return __builtin_bit_cast(float, x << 16); }
DEV float hi16(u32 x) { return __builtin_bit_cast(float, x & 0xFFFF0000u); }
DEV float bfr(float x) { return lo16(pk(x, x)); }
DEV float cosr(float rev) { return __builtin_amdgcn_cosf(rev); }
DEV float sinr(float rev) { return __builtin_amdgcn_sinf(rev); }
DEV f4 mfma16(v4u a, v4u b, f4 c) { return __builtin_amdgcn_mfma_f32_16x16x32_bf16(__builtin_bit_cast(b8, a), __builtin_bit_cast(b8, b), c, 0, 0, 0); }
DEV void mfma16a(f4& c, v4u a, v4u b) { asm volatile("v_mfma_f32_16x16x32_bf16 %0, %1, %2, %0" : "+v"(c) : "v"(a), "v"(b)); }
DEV void mfma16z(f4& c, v4u a, v4u b) { asm volatile("v_mfma_f32_16x16x32_bf16 %0, %1, %2, 0" : "=&v"(c) : "v"(a), "v"(b)); }
DEV void mfma32a(f16v& c, v4u a, v4u b) { asm volatile("v_mfma_f32_32x32x16_bf16 %0, %1, %2, %0" : "+v"(c) : "v"(a), "v"(b)); }
DEV void mfma32z(f16v& c, v4u a, v4u b) { asm volatile("v_mfma_f32_32x32x16_bf16 %0, %1, %2, 0" : "=&v"(c) : "v"(a), "v"(b)); }
template <bool V> DEV void mf2(f4& d0, f4& d1, v4u a0, v4u b0, v4u a1, v4u b1) {
    if (V) asm volatile("s_nop 1\n\tv_mfma_f32_16x16x32_bf16 %0, %2, %3, 0\n\tv_mfma_f32_16x16x32_bf16 %1, %4, %5, 0" : "=&v"(d0), "=&v"(d1) : "v"(a0), "v"(b0), "v"(a1), "v"(b1));
    else asm volatile("v_mfma_f32_16x16x32_bf16 %0, %2, %3, 0\n\tv_mfma_f32_16x16x32_bf16 %1, %4, %5, 0" : "=&v"(d0), "=&v"(d1) : "v"(a0), "v"(b0), "v"(a1), "v"(b1));
}
DEV void mf2acc(f4& d0, f4& d1, v4u a, v4u b0, v4u b1) {
    asm volatile("s_nop 1\n\tv_mfma_f32_16x16x32_bf16 %0, %2, %3, %0\n\tv_mfma_f32_16x16x32_bf16 %1, %2, %4, %1" : "+v"(d0), "+v"(d1) : "v"(a), "v"(b0), "v"(b1));
}
DEV void mf2k(f4& d0, f4& d1, v2u a, v2u b0, v2u b1) {
    asm volatile("s_nop 1\n\tv_mfma_f32_16x16x16_bf16 %0, %2, %3, 0\n\tv_mfma_f32_16x16x16_bf16 %1, %2, %4, 0" : "=&v"(d0), "=&v"(d1) : "v"(a), "v"(b0), "v"(b1));
}
template <int N> DEV void gap(f4& d0, f4& d1) { asm volatile("s_nop %2" : "+v"(d0), "+v"(d1) : "n"(N - 1)); }
DEV f4 mfma16k(v2u a, v2u b, f4 c) { return __builtin_amdgcn_mfma_f32_16x16x16bf16_1k(__builtin_bit_cast(s4, a), __builtin_bit_cast(s4, b), c, 0, 0, 0); }
DEV f16v mfma32(v4u a, v4u b, f16v c) { return __builtin_amdgcn_mfma_f32_32x32x16_bf16(__builtin_bit_cast(b8, a), __builtin_bit_cast(b8, b), c, 0, 0, 0); }
DEV v2u ldtr(u32 off) { return __builtin_bit_cast(v2u, __builtin_amdgcn_ds_read_tr16_b64_v4i16((LAS s4*)(smem + off))); }
DEV v4u ldtr2(u32 off0, u32 off1) { v2u a = ldtr(off0), b = ldtr(off1); return v4u{a.x, a.y, b.x, b.y}; }
template <class T> DEV T& lds(u32 off) { return *(T*)(smem + off); }
DEV rsrc_t mkrs(const void* p, u32 bytes) { return __builtin_amdgcn_make_buffer_rsrc((void*)p, 0, bytes, 0x00020000); }
DEV v4u mkrs4(const void* p, u32 bytes) { unsigned long long a = (unsigned long long)p; return v4u{(u32)a, (u32)(a >> 32) & 0xFFFFu, bytes, 0x00020000u}; }
DEV v4u ld128(rsrc_t r, u32 off) { return __builtin_amdgcn_raw_buffer_load_b128(r, off, 0, 0); }
DEV v4u ld128n(rsrc_t r, u32 off, u32 soff = 0) { return __builtin_amdgcn_raw_buffer_load_b128(r, off, soff, NT); }
DEV v2u ld64n(rsrc_t r, u32 off, u32 soff = 0) { return __builtin_amdgcn_raw_buffer_load_b64(r, off, soff, NT); }
DEV v2u ld64(rsrc_t r, u32 off) { return __builtin_amdgcn_raw_buffer_load_b64(r, off, 0, 0); }
DEV u32 ld16(rsrc_t r, u32 off) { return (u32)__builtin_amdgcn_raw_buffer_load_b16(r, off, 0, 0); }
DEV v4u ld128c(rsrc_t r, u32 off, u32 soff = 0) { return __builtin_amdgcn_raw_buffer_load_b128(r, off, soff, SC1); }
DEV v2u ld64c(rsrc_t r, u32 off) { return __builtin_amdgcn_raw_buffer_load_b64(r, off, 0, SC1); }
DEV void st128c(v4u v, rsrc_t r, u32 off, u32 base = 0) { __builtin_amdgcn_raw_buffer_store_b128(v, r, off + base, 0, SC1); }
DEV void dma16(v4u rs, u32 voff, u32 soff, u32 ldsa) {
    asm volatile("s_mov_b32 m0, %3\n\ts_nop 0\n\tbuffer_load_dwordx4 %0, %1, %2 offen lds" ::"v"(voff), "s"(rs), "s"(soff), "s"(ldsa) : "memory");
}
template <int SOFF, int LOFF, bool COH> DEV void dma16c(v4u rs, u32 voff, u32 sbase, u32 lbase) {
    u32 tmp;
    if (COH) asm volatile("s_add_u32 m0, %4, %6\n\ts_add_u32 %0, %3, %5\n\tbuffer_load_dwordx4 %1, %2, %0 offen sc1 lds" : "=&s"(tmp) : "v"(voff), "s"(rs), "s"(sbase), "s"(lbase), "n"(SOFF), "n"(LOFF) : "memory", "scc");
    else asm volatile("s_add_u32 m0, %4, %6\n\ts_add_u32 %0, %3, %5\n\tbuffer_load_dwordx4 %1, %2, %0 offen lds" : "=&s"(tmp) : "v"(voff), "s"(rs), "s"(sbase), "s"(lbase), "n"(SOFF), "n"(LOFF) : "memory", "scc");
}
template <int LOFF> DEV void dma16d(v4u rs, u32 voff, u32 soff, u32 lbase) {
    asm volatile("s_add_u32 m0, %3, %4\n\ts_nop 0\n\tbuffer_load_dwordx4 %0, %1, %2 offen lds" ::"v"(voff), "s"(rs), "s"(soff), "s"(lbase), "n"(LOFF) : "memory", "scc");
}
DEV void dma4(v4u rs, u32 voff, u32 ldsa) {
    asm volatile("s_mov_b32 m0, %2\n\ts_nop 0\n\tbuffer_load_dword %0, %1, 0 offen lds" ::"v"(voff), "s"(rs), "s"(ldsa) : "memory");
}
template <int N, class F> DEV void sfor(F&& f) { if constexpr (N > 0) { sfor<N - 1>(f); f(std::integral_constant<int, N - 1>{}); } }
DEV void ld128a(v4u& d, v4u rs, u32 voff) { asm volatile("buffer_load_dwordx4 %0, %1, %2, 0 offen sc1" : "=v"(d) : "v"(voff), "s"(rs) : "memory"); }
DEV void ld128a(v4u& d, v4u rs, u32 voff, u32 soff) { asm volatile("buffer_load_dwordx4 %0, %1, %2, %3 offen sc1" : "=v"(d) : "v"(voff), "s"(rs), "s"(soff) : "memory"); }
template <int N, class T> DEV void waitv(T& d) { asm volatile("s_waitcnt vmcnt(%1)" : "+v"(d) : "n"(N) : "memory"); }
#define CW(a, b) (a)
template <int N> DEV void waitvm() { asm volatile("s_waitcnt vmcnt(%0)" ::"n"(N) : "memory"); }
DEV void ld64p(v2u& d, v4u rs, u32 voff, u32 soff) { asm volatile("buffer_load_dwordx2 %0, %1, %2, %3 offen" : "=v"(d) : "v"(voff), "s"(rs), "s"(soff) : "memory"); }
DEV void ld128p(v4u& d, v4u rs, u32 voff) { asm volatile("buffer_load_dwordx4 %0, %1, %2, 0 offen" : "=v"(d) : "v"(voff), "s"(rs) : "memory"); }
DEV void ld32p(u32& d, v4u rs, u32 voff) { asm volatile("buffer_load_dword %0, %1, %2, 0 offen" : "=v"(d) : "v"(voff), "s"(rs) : "memory"); }
DEV void ld32a(u32& d, v4u rs, u32 voff) { asm volatile("buffer_load_dword %0, %1, %2, 0 offen sc1" : "=v"(d) : "v"(voff), "s"(rs) : "memory"); }
template <int N> DEV void prio() { __builtin_amdgcn_s_setprio(N); }
#define VM0() asm volatile("s_waitcnt vmcnt(0)" ::: "memory")
#define BAR() do { asm volatile("s_waitcnt lgkmcnt(0)" ::: "memory"); __builtin_amdgcn_s_barrier(); asm volatile("" ::: "memory"); } while (0)
#define SCHED() __builtin_amdgcn_sched_barrier(0)
#define PIN(x) asm volatile("" : "+v"(x))
#define RETRY(BAD, LOAD) do { if (__builtin_expect(anylane(BAD), 0)) { u32 sp_ = 0; \
        _Pragma("nounroll") do { asm volatile("" ::: "memory"); LOAD; } while (anylane(BAD) && ++sp_ < SPIN_CAP); } } while (0)
template <int N> DEV void nap() { if (N > 0) __builtin_amdgcn_s_sleep(N); }
DEV u32 umax3(u32 a, u32 b, u32 c) { return max(max(a, b), c); }
DEV u32 vmax(v4u v, u32 m) { return umax3(umax3(m, v.x, v.y), v.z, v.w); }
template <int N> DEV u32 vmaxn(const v4u (&v)[N]) { u32 m = 0;
#pragma unroll
    for (int i = 0; i < N; ++i) m = vmax(v[i], m);
    return m; }
DEV int fresh_lane() { int x; asm volatile("v_mbcnt_lo_u32_b32 %0, -1, 0\n\tv_mbcnt_hi_u32_b32 %0, -1, %0" : "=v"(x)); return x; }
DEV bool anylane(bool b) { return __builtin_amdgcn_ballot_w64(b) != 0ull; }

template <int CTRL, int RM> DEV float dppadd(float v) {
    return v + __builtin_bit_cast(float, __builtin_amdgcn_update_dpp(0, __builtin_bit_cast(int, v), CTRL, RM, 0xF, true));
}
template <int CTRL> DEV float dppget(float v) { return __builtin_bit_cast(float, __builtin_amdgcn_update_dpp(0, __builtin_bit_cast(int, v), CTRL, 0xF, 0xF, true)); }
DEV float swapadd16(float v) { const u32 x = __builtin_bit_cast(u32, v); const auto r = __builtin_amdgcn_permlane16_swap(x, x, false, false); const u32 a = r[0], b = r[1]; return __builtin_bit_cast(float, a) + __builtin_bit_cast(float, b); }
DEV float swapadd32(float v) { const u32 x = __builtin_bit_cast(u32, v); const auto r = __builtin_amdgcn_permlane32_swap(x, x, false, false); const u32 a = r[0], b = r[1]; return __builtin_bit_cast(float, a) + __builtin_bit_cast(float, b); }
DEV float wave_sum(float v) {
    v = dppadd<0xB1, 0xF>(v); v = dppadd<0x4E, 0xF>(v); v = dppadd<0x141, 0xF>(v); v = dppadd<0x140, 0xF>(v);
    v = dppadd<0x142, 0xF>(v); v = dppadd<0x143, 0xF>(v);
    return __builtin_bit_cast(float, __builtin_amdgcn_readlane(__builtin_bit_cast(int, v), 63));
}
DEV f2 wave_sum2(float a, float b) {
#define DPP2(C) do { a = dppadd<C, 0xF>(a); b = dppadd<C, 0xF>(b); SCHED(); } while (0)
    SCHED(); DPP2(0xB1); DPP2(0x4E); DPP2(0x141); DPP2(0x140); DPP2(0x142); DPP2(0x143);
    return f2{__builtin_bit_cast(float, __builtin_amdgcn_readlane(__builtin_bit_cast(int, a), 63)), __builtin_bit_cast(float, __builtin_amdgcn_readlane(__builtin_bit_cast(int, b), 63))};
}
DEV f2 cmul(f2 z, f2 w) {
    f2 r;
    asm("v_pk_mul_f32 %0, %1, %2 op_sel_hi:[1,0]\n\tv_pk_fma_f32 %0, %1, %2, %0 op_sel:[1,1,0] op_sel_hi:[0,1,1] neg_lo:[0,1,0]" : "=&v"(r) : "v"(z), "v"(w));
    return r;
}
DEV float silu(float x) { return x * __builtin_amdgcn_rcpf(1.0f + __builtin_amdgcn_exp2f(-1.44269504089f * x)); }

DEV void fwd_pair(float rev0, float drev, v4u& R, v4u& I) {
#pragma unroll
    for (int r = 0; r < 4; ++r) { const float a = rev0 + r * drev, c = cosr(a), s = sinr(a); R[r] = pk(c, s); I[r] = pk(-s, c); }
}
DEV v4u pk4(f4 a, f4 b) { return v4u{pk(a[0], b[0]), pk(a[1], b[1]), pk(a[2], b[2]), pk(a[3], b[3])}; }
DEV v4u twid_pack(f4 re, f4 im, const f4& c, const f4& s) {
    f4 a = re * c + im * s, b = im * c - re * s;
    PIN(a); PIN(b);
    return pk4(a, b);
}
DEV int row_of(int L, int rho) { return 128 * (4 * ((L >> 3) & 3) + (rho & 3)) + 8 * (2 * (L & 7) + (rho >> 2)) + (L >> 5); }

#define STAMP(i)
#define STAMPC(i)
#define STAMP_FLUSH()

struct Args { const u16 *pwm, *pc; unsigned char* ws; const u16 *px, *pwr, *pwi, *pw1, *pw3, *pw2; u16* pout; };
typedef u32 s16u __attribute__((ext_vector_type(16)));
DEV unsigned char* mkptr(u32 lo, u32 hi) { return (unsigned char*)(((unsigned long long)hi << 32) | lo); }

__global__ void __launch_bounds__(512, 2) fused_kernel(Args) {
    s16u ka; v4u kb4;
    asm volatile("s_load_dwordx16 %0, %2, 0x0\n\ts_load_dwordx4 %1, %2, 0x40" : "=&s"(ka), "=&s"(kb4) : "s"(__builtin_amdgcn_kernarg_segment_ptr()));
    const int t = threadIdx.x, l = t & 63, li = l & 15, g = l >> 4;
    const int w = __builtin_amdgcn_readfirstlane(t >> 6);
    const int bid = blockIdx.x, L = (bid & 7) * 32 + (bid >> 3);
    if (w == 0) {
        unsigned long long pcv; u32 len;
        asm volatile("s_getpc_b64 %0\n.Lpcref%=:\n\ts_mov_b32 %1, .Lcode_tail-.Lpcref%=" : "=s"(pcv), "=s"(len));
        dma16(mkrs4((const void*)pcv, len), l < 12 ? (l * 32 + (L & 31)) * 128 : OOB, 0, L_SCRAP);
    }
    {
        const v4u rt = mkrs4(&g_tab, sizeof(Tab));
        asm volatile("s_nop 4" ::"s"(w));
        dma16(rt, l * 16, w * 1024, L_TAB + w * 1024); dma16(rt, l * 16, w * 1024 + 8192, L_TAB + 8192 + w * 1024);
    }
    const int kapB = L ^ 3;
    u32 x1_w = WS_X1 + L * XSTR + t * 16, x2_w = WS_X2 + kapB * XSTR + t * 16, e0a = l * 6144 + 8 * L;
    const v4u sent4 = {SENT, SENT, SENT, SENT};
    u32 wro, xso = row_of(L, w) * 1024, wla = L_WR + WRB(w);
    {
        const int tt = 2 * L + (w >> 2), j = w & 3;
        const int pos = 32 * (tt >> 6) + 2 * (tt & 15) + (j >> 1), kb = 2 * ((tt >> 4) & 3) + (j & 1);
        const int k = ((pos >> 2) & 15) + 16 * (4 * (pos >> 6) + (pos & 3)) + 256 * kb, km = (2048 - k) & 2047;
        u32 o0 = ((k * 514) & ~3) + 16 * l, om = ((km * 514) & ~3) + (l - 33) * 512;
        PIN(o0); PIN(om);
        om = l < 35 ? om : OOB; wro = l < 33 ? o0 : om;
    }
    PIN(e0a); PIN(wro); asm volatile("" : "+s"(xso), "+s"(wla));
    SCHED();
    asm volatile("s_waitcnt lgkmcnt(0)" : "+s"(ka), "+s"(kb4));
    unsigned char *const pwm = mkptr(ka[0], ka[1]), *const pc = mkptr(ka[2], ka[3]), *const ws = mkptr(ka[4], ka[5]), *const px = mkptr(ka[6], ka[7]),
                  *const pwr = mkptr(ka[8], ka[9]), *const pwi = mkptr(ka[10], ka[11]), *const pw1 = mkptr(ka[12], ka[13]), *const pw3 = mkptr(ka[14], ka[15]),
                  *const pw2 = mkptr(kb4[0], kb4[1]), *const pout = mkptr(kb4[2], kb4[3]);
    STAMP(0);
    STAMPC(60);
    const rsrc_t rws = mkrs(ws, WS_END);
    const v4u rws4 = mkrs4(ws, WS_END);
    const f4 z4 = {0.f, 0.f, 0.f, 0.f};
    const float fl = (float)li, fg4 = (float)(4 * g), fw = (float)w;
    v4u GR, GI, HR, HI;
    f4 bc, bs, bc2, bs2;
#define TAB(i) lds<v4u>(L_TAB + (i) * 1024 + l * 16)
#define CONSTS_B() do { \
        _Pragma("unroll") for (int r = 0; r < 4; ++r) { \
            const float a3 = (8.f * (fg4 + r) + fw) * fl * (1.f / 2048), a4 = (8.f * fl + fw) * (fg4 + r) * (1.f / 2048); \
            bc[r] = cosr(a3); bs[r] = sinr(a3); bc2[r] = cosr(a4); bs2[r] = sinr(a4); } \
        fwd_pair((8.f * fg4 + fw) * fl * (1.f / 128), fl * (1.f / 16), GR, GI); \
        fwd_pair((8.f * fl + fw) * fg4 * (1.f / 128), (8.f * fl + fw) * (1.f / 128), HR, HI); \
        PIN(GR); PIN(GI); PIN(HR); PIN(HI); PIN(bc); PIN(bs); PIN(bc2); PIN(bs2); } while (0)
    u32 tagv; ld32a(tagv, rws4, WS_EPOCH);
#define ROWS() do { dma16(mkrs4(px, 2048 * 1024), 16 * l, xso, L_XS + w * 1024); \
        dma16(mkrs4(pwr, 2048 * 514), wro, 0, wla); dma16(mkrs4(pwi, 2048 * 514), wro, 0, wla + 1024); } while (0)
    v2u e0[8]; v4u cv;
    if (w == GW) {
        ld128p(cv, mkrs4(pc, 1024), 16 * l);
        const v4u rwm = mkrs4(pwm, 512 * 3072 * 2);
#pragma unroll
        for (int i = 0; i < 8; ++i) ld64p(e0[i], rwm, e0a, ((i + L) & 7) * (64 * 6144));
        ROWS();
    }
    SCHED();
    STAMP(33);
    const int P = (L >> 7) * 8 + ((L >> 2) & 7), mem = ((L >> 5) & 3) * 4 + (L & 3);
    const bool act = (L & 3) < 3 || (((L >> 5) ^ (L >> 2)) & 1);
    const int cbk = 7 * ((L >> 6) & 1) + ((L & 3) < 3 ? 4 * ((L >> 5) & 1) + (L & 3) : 3);
    const int si = 2 * P + ((L >> 6) & 1);
    v4u rs1 = act ? mkrs4(pw1, 512 * 1792 * 2) : mkrs4(pwm, 512 * 3072 * 2), rs3 = act ? mkrs4(pw3, 512 * 1792 * 2) : rs1;
    u32 wv1 = (4 * w + g) * 3584 + cbk * 256 + (((li >> 1) ^ g) * 2 + (l & 1)) * 16, wv3 = wv1 ^ 128;
    if (!act) { wv1 = (8 * w + (l >> 3)) * 6144 + 2048 + si * 128 + ((((l >> 1) & 3) ^ (((l >> 4) & 1) | ((w & 1) << 1))) * 2 + (l & 1)) * 16; wv3 = wv1 + 64 * 6144; }
    const u32 pstr = act ? 114688 : 128 * 6144;
    u32 dso[6], dla[6];
#define W13_PREP(first, nb) do { int p_ = act ? (L >> 2) & 7 : 0; p_ = p_ >= (nb) ? p_ - (nb) : p_; _Pragma("unroll") for (int i = 0; i < (nb); ++i) { \
        dso[i] = ((first) + p_) * pstr; dla[i] = L_BIG + p_ * 16384 + w * 1024; p_ = p_ + 1 == (nb) ? 0 : p_ + 1; \
        asm volatile("" : "+s"(dso[i]), "+s"(dla[i])); } } while (0)
#define W13_PC(j) dma16d<((j) & 1) * 8192>(((j) & 1) ? rs3 : rs1, ((j) & 1) ? wv3 : wv1, dso[(j) >> 1], dla[(j) >> 1])
#define NX(a) (a)
#define W13_RANGE(a, b) sfor<(b) - (a)>([&](auto i_) { W13_PC((a) + i_.value); })
#define SPR(k, base, n) do { if constexpr ((k) < (n)) { SCHED(); W13_PC((base) + (k)); SCHED(); } } while (0)
#define SPRG(k, base, n, d0, d1, st) do { SPR(k, base, n); gap<(st) - (NX((k) < (n)) ? 3 : 0)>(d0, d1); } while (0)
    u32 trd = (4 * (t & 1)) * 1040 + (t >> 1) * 4;
    u32 rowo = w * 1040 + l * 16, wgo;
    {
        const int kap = t >> 1, k1 = (kap >> 2) & 15, kk = 4 * (kap >> 6) + (kap & 3);
        const int ka_ = kk < 8 ? k1 + 32 * kk : 512 - k1 - 32 * kk, k2 = (k1 == 0 && kk >= 8) ? 32 * kk - 240 : ka_;
        wgo = L_WR + (t & 1) * WRB(4) + (L & 1) * 2 + k2 * 2;
    }
    u32 tk_w = l == 0 ? WS_TK + w * 1024 + 4 * L : OOB, mrd = L_MOD0 + 8 * l, wt_w = t < 2 ? OOB : x1_w + (WS_WT - WS_X1);
    W13_PREP(0, NB1);
    PIN(x1_w); PIN(x2_w); PIN(trd); PIN(rowo); PIN(wgo); PIN(tk_w); PIN(mrd); PIN(wt_w); PIN(wv1); PIN(wv3);
    SCHED();
    STAMP(1);
    if (w == GW) {
        u32 g0a = (l & 61) == 0 ? WS_G0 + 16 * L + 4 * l : OOB;
        float one = L >= 128 ? 1.f : 0.f;
        PIN(g0a); PIN(one);
        waitv<11>(cv); PIN(tagv);
        f4 s0, s1;
#pragma unroll
        for (int i = 0; i < 2; ++i) {
            s0[2 * i] = bfr(silu(lo16(cv[i]))); s0[2 * i + 1] = bfr(silu(hi16(cv[i])));
            s1[2 * i] = bfr(silu(lo16(cv[2 + i]))); s1[2 * i + 1] = bfr(silu(hi16(cv[2 + i])));
        }
        lds<f4>(L_MOD1 + 32 * l) = s0; lds<f4>(L_MOD1 + 32 * l + 16) = s1;
        lds<v4u>(L_MOD1 + 2048 + 16 * l) = v4u{pk(s0[0], s0[1]), pk(s0[2], s0[3]), pk(s1[0], s1[1]), pk(s1[2], s1[3])};
        asm volatile("s_waitcnt lgkmcnt(0)" ::: "memory");
        float sc[8];
#pragma unroll
        for (int i = 0; i < 8; ++i) sc[i] = lds<float>(L_MOD1 + ((i + L) & 7) * 256 + 4 * l);
        u32 tg = tagv + 1u;
        asm volatile("" : "+v"(sc[0]), "+v"(sc[1]), "+v"(sc[2]), "+v"(sc[3]), "+v"(sc[4]), "+v"(sc[5]), "+v"(sc[6]), "+v"(sc[7]), "+v"(tg));
        SCHED();
        STAMP(38);
        f2 a0 = {0.f, 0.f}, a1 = {0.f, 0.f};
        sfor<8>([&](auto i) { waitv<10 - i.value>(e0[i.value]);
            a0 += sc[i.value] * f2{lo16(e0[i.value].x), hi16(e0[i.value].x)}; a1 += sc[i.value] * f2{lo16(e0[i.value].y), hi16(e0[i.value].y)}; SCHED(); });
        const bool o1 = l & 1, o2 = l & 2;
        const float b0 = (o1 ? a0.y : a0.x) + dppget<0xB1>(o1 ? a0.x : a0.y), b1 = (o1 ? a1.y : a1.x) + dppget<0xB1>(o1 ? a1.x : a1.y);
        float c0 = (o2 ? b1 : b0) + dppget<0x4E>(o2 ? b0 : b1);
        c0 = dppadd<0x124, 0xF>(c0); c0 = dppadd<0x128, 0xF>(c0);
        c0 = swapadd16(c0); c0 = swapadd32(c0);
        c0 = bfr(c0) + one;
        __builtin_amdgcn_raw_buffer_store_b64(v2u{pk(c0, dppget<0xB1>(c0)), tg}, rws, g0a, 0, SC1);
        STAMP(2);
    } else { ROWS(); SCHED(); CONSTS_B(); }
    SCHED();
    __builtin_amdgcn_s_barrier();
    STAMP(41);
    constexpr int NF = F2P == 0 ? 2 : 1;
    st128c(sent4, rws, x1_w);
    if (F2P == 0) st128c(sent4, rws, x2_w);
    if (w == GW) waitvm<3 + NF>(); else waitvm<2 + NF>();
    f2 xh[4]; v2u xc0, xc1; float mean1;
    {
        const v4u xa = ldtr2(L_XS + w * 1024 + 8 * l, L_XS + w * 1024 + 512 + 8 * l);
        xc0 = lds<v2u>(L_XS + w * 1024 + 64 * li + 8 * g); xc1 = lds<v2u>(L_XS + w * 1024 + 64 * li + 32 + 8 * g);
        f2 v[4];
#pragma unroll
        for (int i = 0; i < 4; ++i) v[i] = f2{lo16(xa[i]), hi16(xa[i])};
        const f2 s = (v[0] + v[1]) + (v[2] + v[3]);
        mean1 = wave_sum(s.x + s.y) * (1.f / 512);
        f2 q = {0.f, 0.f};
#pragma unroll
        for (int i = 0; i < 4; ++i) { v[i] -= mean1; q += v[i] * v[i]; }
        const float rstd = __builtin_amdgcn_rsqf(wave_sum(q.x + q.y) * (1.f / 512) + 1e-5f);
#pragma unroll
        for (int i = 0; i < 4; ++i) { xh[i] = v[i] * rstd; PIN(xh[i]); }
    }
    PIN(tagv);
    const u32 tag = __builtin_amdgcn_readfirstlane(tagv) + 1u;
    SCHED();
    STAMP(39);
    v4u m0[4];
#define G0_LOAD do { _Pragma("unroll") for (int i = 0; i < 4; ++i) m0[i] = ld128c(rws, l * 16 + i * 1024, WS_G0); } while (0)
    if (w == GW) {
        waitvm<1 + NF>();
        nap<D_E0>(); ld128a(m0[0], rws4, WS_G0 + l * 16); ld128a(m0[1], rws4, WS_G0 + 1024 + l * 16); ld128a(m0[2], rws4, WS_G0 + 2048 + l * 16); ld128a(m0[3], rws4, WS_G0 + 3072 + l * 16);
    } else waitvm<NF>();
    BAR();
    STAMP(40);
    v4u wo4;
#pragma unroll
    for (int j = 0; j < 4; ++j) wo4[j] = (u32)lds<u16>(wgo + j * WRS) | ((u32)lds<u16>(wgo + j * WRS + 1024) << 16);
    const v4u aBR = TAB(T_aBR), aBI = TAB(T_aBI), FR = TAB(T_FR), aAI = TAB(T_aAI), aSR = TAB(T_aSR), aSI = TAB(T_aSI);
    const f4 tc = __builtin_bit_cast(f4, (v4u)TAB(T_tc)), ts = __builtin_bit_cast(f4, (v4u)TAB(T_ts));
    SCHED();
    st128c(wo4, rws, wt_w);
    if (w == GW) waitvm<4 + NF>(); else waitvm<NF>();
    __builtin_amdgcn_raw_buffer_store_b32(tag, rws, tk_w, 0, SC1);
    tk_w += WS_TKB - WS_TK; PIN(tk_w);
    if (F2P == 1) st128c(sent4, rws, x2_w);
    if (w == 0) {
        const u32 b = L_WR + WRB(l & 7) + (L & 1) * 2;
#define WE(off) lo16((u32)lds<u16>(b + (off)))
        const float h0r = WE(0) + WE(528), h0i = WE(1024) - WE(1024 + 528), h1r = WE(512) + WE(544), h1i = WE(1024 + 512) - WE(1024 + 544);
        const float sg = (l & 8) ? -0.25f : 0.25f;
        __builtin_amdgcn_raw_buffer_store_b32(pk(0.25f * h0r + sg * h1r, 0.25f * h0i + sg * h1i), rws, l < 16 ? WS_WT + L * XSTR + (l >> 3) * 8192 + (l & 7) * 4 : OOB, 0, SC1);
    }
    SCHED();
    STAMP(3);
    W13_RANGE(0, N1);
#define W13_TOUCH(p0, np) do { if (act && w >= 4 && 4 * (w - 4) < (np)) { const int pp = 4 * (w - 4) + (l >> 4); \
        const u32 o_ = pp < (np) ? (((p0) + pp) * 32 + ((L >> 2) & 7) * 4 + ((l >> 1) & 3)) * 3584 + cbk * 256 + (l & 1) * 128 : OOB; \
        dma4(rs1, (l & 8) ? OOB : o_, L_SCRAP); dma4(rs3, (l & 8) ? o_ : OOB, L_SCRAP); } } while (0)
    if (TOUCH == 1) W13_TOUCH(NB1, NB2 + NB3);
    if (TOUCH == 2) W13_TOUCH(NB1, NB2);
    SCHED();
    STAMP(32);
    if (w == GW) {
        waitvm<2 + F2P + NX(N1)>();
        PIN(m0[0]); PIN(m0[1]); PIN(m0[2]); PIN(m0[3]);
        RETRY(((m0[0].y ^ tag) | (m0[0].w ^ tag) | (m0[1].y ^ tag) | (m0[1].w ^ tag) | (m0[2].y ^ tag) | (m0[2].w ^ tag) | (m0[3].y ^ tag) | (m0[3].w ^ tag)) != 0, G0_LOAD);
#pragma unroll
        for (int i = 0; i < 4; ++i) lds<v2u>(mrd + i * 512) = v2u{m0[i].x, m0[i].z};
    }
    BAR();
    STAMP(4);

    {
        const v4u sa = ldtr2(mrd, mrd + 512), ca = ldtr2(mrd + 1024, mrd + 1536);
        SPR(0, N1, NA); SPR(1, N1, NA);
        f4 yr, yi;
        mf2<false>(yr, yi, sa, aBR, sa, aBI);
        v4u ha;
#pragma unroll
        for (int i = 0; i < 4; ++i) { const f2 h = xh[i] * f2{lo16(ca[i]), hi16(ca[i])}; ha[i] = pk(h.x, h.y); }
        mf2acc(yr, yi, ha, aBR, aBI);
        SPRG(2, N1, NA, yr, yi, 8);
        const v4u yb = twid_pack(yr, yi, tc, ts);
        f4 xr, xi, sr, si4;
        mf2<true>(xr, xi, FR, yb, aAI, yb); mf2<false>(sr, si4, aSR, yb, aSI, yb);
        SPRG(3, N1, NA, xr, xi, 6);
        v4u o1 = pk4(xr, xi); PIN(o1);
        asm volatile("" : "+v"(sr), "+v"(si4));
        v4u o2 = pk4(sr, si4); PIN(o2);
        v4u o = li ? o1 : o2;
        lds<v4u>(L_T1 + rowo) = o;
        SCHED();
        if (TOUCH && act && w >= 4 && 4 * (w - 4) < (TOUCH == 1 ? NB2 + NB3 : NB2)) waitvm<2 + NX(N1 + NA)>(); else waitvm<NX(N1 + NA)>();
        __builtin_amdgcn_raw_buffer_store_b32(tag, rws, tk_w, 0, SC1);
        BAR();
#pragma unroll
        for (int j = 0; j < 4; ++j) o[j] = lds<u32>(L_T1 + trd + j * 1040);
        st128c(o, rws, x1_w);
    }
    SCHED();
    STAMP(5);
    __builtin_amdgcn_s_barrier();
    W13_RANGE(N1 + NA, 2 * NB1);
    if (L == 0 && w == 7) __builtin_amdgcn_raw_buffer_store_b32(tag, rws, l == 0 ? WS_EPOCH : OOB, 0, SC1);
    nap<D_TK>();
    u32 tk;
#define TK_LOAD tk = __builtin_amdgcn_raw_buffer_load_b32(rws, (t >> 1) * 4, WS_TK + (L >> 5) * 1024, SC1)
    u32 tk_r = WS_TK + (L >> 5) * 1024 + (t >> 1) * 4;
    ld32a(tk, rws4, tk_r);
    const u32 h2_w = (P * 128 + mem * 8 + w) * 1024 + (4 * li + g) * 16;
    u32 h2_a = h2_w + WS_H2;
    st128c(sent4, rws, h2_a);
    tk_r += WS_TKB - WS_TK;
    u32 xrd = (t >> 1) * XSTR + kapB * 32 + (t & 1) * 16, x1_r = WS_X1 + xrd, wt_r = WS_WT + xrd, wb_r = kapB == 0 ? WS_WT + xrd + 8192 : OOB;
    u32 wtr = L_BIG + (g & 1) * 8192 + (4 * (l >> 5) + ((l >> 2) & 3)) * 256 + ((w ^ ((l >> 2) & 3) ^ ((g & 1) * 4)) * 32) + (((l & 1) << 1) | ((l >> 1) & 1)) * 8;
    u32 e1w = w * 1088 + l * 16, eo = 2 * g * 1088 + (32 * w + 2 * li) * 4;
    u32 pm0 = 0, pm1 = 0;
    if (kapB == 0) {
#pragma unroll
        for (int jb = 0; jb < 2; ++jb) {
            const int pos = 32 * w + 2 * li + jb, q = ((pos >> 2) & 15) + 16 * (4 * (pos >> 6) + (pos & 3)), qm = (256 - q) & 255;
            (jb ? pm1 : pm0) = L_E1 + 2 * g * 1088 + (64 * (qm >> 6) + 4 * (qm & 15) + ((qm >> 4) & 3)) * 4;
        }
    }
    PIN(x1_r); PIN(wt_r); PIN(wb_r); PIN(wtr); PIN(e1w); PIN(eo); PIN(pm0); PIN(pm1); PIN(tk_r);
    if (w == GW) CONSTS_B();
    if (!act) { rs1.z = 0; rs3.z = 0; }
    asm volatile("" : "+s"(rs1), "+s"(rs3));
    W13_PREP(NB1, NB2);
    const v4u FI = TAB(T_FI), A81 = TAB(T_A8);
    const v2u A8 = {A81.x, A81.y}, A1 = {A81.z, A81.w};
    waitvm<1>(); PIN(tk);
    RETRY(tk != tag, TK_LOAD);
    STAMP(6);
    nap<D_E1>();
    v4u zin, wf4, wb4;
    ld32a(tk, rws4, tk_r);
    ld128a(zin, rws4, x1_r);
    BAR();
    v4u wf[32];
#define W13_READ(first, nb) do { u32 hi_ = wtr + 65536; PIN(hi_); _Pragma("unroll") for (int p = 0; p < (nb); ++p) { \
        const u32 o_ = (p < 4 ? wtr : hi_) + (p & 3) * 16384; \
        wf[2 * ((first) + p)] = ldtr2(o_, o_ + 4096); wf[2 * ((first) + p) + 1] = ldtr2(o_ + 2048, o_ + 6144); } } while (0)
    if (act) W13_READ(0, NB1);
    else if (w < 4) {
        u32 ao = L_BIG + (8 * g + ((l >> 2) & 3)) * 128 + (w ^ ((l >> 3) & 3)) * 32 + (l & 3) * 8, bo = L_MOD1 + 2048 + g * 16;
        PIN(ao); PIN(bo);
        f4 d = z4;
#pragma unroll
        for (int k = 0; k < 16; ++k) d = mfma16(ldtr2(ao + k * 4096, ao + k * 4096 + 512), lds<v4u>(bo + k * 64), d);
        const float one = (si >> 3) == 2 ? 1.f : 0.f;
        st128c(v4u{pk(bfr(d[0]) + one, bfr(d[1]) + one), tag, pk(bfr(d[2]) + one, bfr(d[3]) + one), tag}, rws, li ? OOB : WS_G1 + si * 256 + w * 64 + g * 16);
    }
    BAR();
    waitv<1>(tk);
    if (__builtin_expect(anylane(tk != tag), 0)) { waitv<0>(zin); RETRY(vmax(zin, 0) == SENT, { ld128a(zin, rws4, x1_r); waitv<0>(zin); }); }
    ld128a(wf4, rws4, wt_r);
    ld128a(wb4, rws4, wb_r);
    W13_RANGE(0, ND);
    waitv<NX(ND) + 2>(zin);
    STAMP(7);

    {
        constexpr int NS = NSB;
        static_assert(NS <= 8 && ND + NS <= 2 * NB2, "sprinkle slots");
        f4 re, im;
        mf2<false>(re, im, zin, FR, zin, FI);
        if (__builtin_expect(anylane(vmax(zin, 0) == SENT), 0)) {
            RETRY(vmax(zin, 0) == SENT, { ld128a(zin, rws4, x1_r); waitv<0>(zin); });
            mf2<false>(re, im, zin, FR, zin, FI); gap<8>(re, im);
        }
        SPRG(0, ND, NS, re, im, 5);
        v4u zb = twid_pack(re, im, bc, bs);
        mf2<true>(re, im, GR, zb, GI, zb);
        SPRG(1, ND, NS, re, im, 8);
        lds<v4u>(L_E1 + e1w) = pk4(re, im);
        SPR(2, ND, NS);
        BAR();
        const v2u u0 = lds<v2u>(L_E1 + eo), u1 = lds<v2u>(L_E1 + eo + 1088);
        SPR(3, ND, NS);
        f4 d0, d1;
        mf2k(d0, d1, A8, v2u{u0.x, u1.x}, v2u{u0.y, u1.y});
        waitv<NX(ND + (NS < 4 ? NS : 4))>(wf4); PIN(wb4);
        f2 wq[4];
#pragma unroll
        for (int j = 0; j < 4; ++j) wq[j] = f2{lo16(wf4[j]), hi16(wf4[j])};
        asm volatile("" : "+v"(wq[0]), "+v"(wq[1]), "+v"(wq[2]), "+v"(wq[3]), "+v"(d0), "+v"(d1));
        f2 f[4] = {cmul(f2{d0[0], d0[1]}, wq[0]), cmul(f2{d0[2], d0[3]}, wq[1]), cmul(f2{d1[0], d1[1]}, wq[2]), cmul(f2{d1[2], d1[3]}, wq[3])};
        if (kapB == 0) {
            const v2u um0 = {lds<u32>(pm0), lds<u32>(pm0 + 1088)}, um1 = {lds<u32>(pm1), lds<u32>(pm1 + 1088)};
            f4 mz = mfma16k(A8 ^ 0x80000000u, um0, z4), m0_, m1_;
            mf2k(m0_, m1_, A1, um0, um1); gap<8>(m0_, m1_);
            if ((w | li) == 0) m0_ = mz;
            f[0] += cmul(f2{m0_[0], m0_[1]}, f2{lo16(wb4[0]), hi16(wb4[0])}); f[1] += cmul(f2{m0_[2], m0_[3]}, f2{lo16(wb4[1]), hi16(wb4[1])});
            f[2] += cmul(f2{m1_[0], m1_[1]}, f2{lo16(wb4[2]), hi16(wb4[2])}); f[3] += cmul(f2{m1_[2], m1_[3]}, f2{lo16(wb4[3]), hi16(wb4[3])});
        }
        mf2k(d0, d1, A8, v2u{pk(f[0].y, f[0].x), pk(f[1].y, f[1].x)}, v2u{pk(f[2].y, f[2].x), pk(f[3].y, f[3].x)});
        SPRG(4, ND, NS, d0, d1, 8);
        lds<v2u>(L_E2 + eo) = v2u{pk(d0[0], d0[1]), pk(d1[0], d1[1])};
        lds<v2u>(L_E2 + eo + 1088) = v2u{pk(d0[2], d0[3]), pk(d1[2], d1[3])};
        SPR(5, ND, NS);
        BAR();
        zb = lds<v4u>(L_E2 + e1w);
        SPR(6, ND, NS);
        mf2<false>(re, im, zb, HR, zb, HI);
        SPRG(7, ND, NS, re, im, 8);
        zb = twid_pack(re, im, bc2, bs2);
        mf2<true>(re, im, FR, zb, FI, zb);
        gap<8>(re, im);
        st128c(pk4(im, re), rws, x2_w);
    }
    SCHED();
    STAMP(8);
    __builtin_amdgcn_s_barrier();
    v4u m1;
    ld128a(m1, rws4, WS_G1 + t * 16);
    W13_RANGE(ND + NSB, 2 * NB2);
    if (TOUCH == 2) W13_TOUCH(NB1 + NB2, NB3);
    const v4u cBR = TAB(T_cBR), cBI = TAB(T_cBI), cSR = TAB(T_cSR), cSI = TAB(T_cSI), cB0 = TAB(T_cB0), cB1 = TAB(T_cB1);
    u32 x2_r = WS_X2 + (xrd ^ 96);
    u32 m1w = L_MF + (t >> 7) * 2048 + ((t >> 2) & 1) * 1024 + (t & 3) * 256 + ((t >> 3) & 15) * 16;
#define UNP(v) f2{lo16(v), hi16(v)}
    f2 xp[4] = {UNP(xc0.x) - mean1, UNP(xc0.y) - mean1, UNP(xc1.x) - mean1, UNP(xc1.y) - mean1};
    PIN(x2_r); PIN(h2_a); PIN(m1w); PIN(xp[0]); PIN(xp[1]); PIN(xp[2]); PIN(xp[3]);
    W13_PREP(NB1 + NB2, NB3);
    waitv<0>(m1);
    STAMP(9);
    nap<D_E2>();
    ld128a(zin, rws4, x2_r);
    RETRY(((m1.y ^ tag) | (m1.w ^ tag)) != 0, { ld128a(m1, rws4, WS_G1 + t * 16); waitv<0>(m1); });
    lds<f4>(m1w) = f4{lo16(m1.x), hi16(m1.x), lo16(m1.z), hi16(m1.z)};
    BAR();
    if (act) W13_READ(NB1, NB2);
    const f4 ga0 = lds<f4>(L_MF + l * 16), ga1 = lds<f4>(L_MF + 1024 + l * 16), sb0 = lds<f4>(L_MF + 2048 + l * 16), sb1 = lds<f4>(L_MF + 3072 + l * 16),
             cb0 = lds<f4>(L_MF + 4096 + l * 16), cb1 = lds<f4>(L_MF + 5120 + l * 16);
    BAR();
    W13_RANGE(0, NG);
    waitv<NX(NG)>(zin);
    STAMP(10);

    v4u xo;
    {
        constexpr int NS = NSC;
        static_assert(NS <= 6 && NG + NS <= 2 * NB3, "sprinkle slots");
#define ZPUT do { _Pragma("unroll") for (int j = 0; j < 4; ++j) lds<u32>(L_T0 + trd + j * 1040) = zin[j]; } while (0)
        ZPUT;
        SCHED();
        if (__builtin_expect(anylane(vmax(zin, 0) == SENT), 0)) { RETRY(vmax(zin, 0) == SENT, { ld128a(zin, rws4, x2_r); waitv<0>(zin); }); ZPUT; }
        SPR(0, NG, NS);
        BAR();
        const v4u va = lds<v4u>(L_T0 + rowo);
        SPR(1, NG, NS); SPR(2, NG, NS);
        f4 sr, si4, ur, ui;
        mf2<false>(sr, si4, va, cSR, va, cSI); mf2<false>(ur, ui, va, cBR, va, cBI);
        SPRG(3, NG, NS, ur, ui, 8);
        asm volatile("" : "+v"(sr), "+v"(si4));
        if (g == 0) { ur[0] = sr[0]; ui[0] = si4[0]; }
        f4 tr = ur * tc - ui * ts, ti = ui * tc + ur * ts;
        PIN(tr); PIN(ti);
        const v4u ta = pk4(tr, ti);
        f4 y0, y1;
        mf2<true>(y0, y1, ta, cB0, ta, cB1);
        SPRG(4, NG, NS, y0, y1, 8);
        const f4 v0 = f4{xp[0].x, xp[0].y, xp[1].x, xp[1].y} + ga0 * y0, v1 = f4{xp[2].x, xp[2].y, xp[3].x, xp[3].y} + ga1 * y1;
        const f4 p1 = v0 + v1, p2 = v0 * v0 + v1 * v1;
        const f2 s1 = f2{p1[0], p1[1]} + f2{p1[2], p1[3]}, s2 = f2{p2[0], p2[1]} + f2{p2[2], p2[3]};
        SPR(5, NG, NS);
        const f2 ss = wave_sum2(s1.x + s1.y, s2.x + s2.y) * (1.f / 512);
        const float m2 = ss.x, rstd = __builtin_amdgcn_rsqf(fmaxf(ss.y - m2 * m2, 0.f) + 1e-5f);
        const f4 h0 = ((v0 - m2) * cb0) * rstd + sb0, h1 = ((v1 - m2) * cb1) * rstd + sb1;
        st128c(v4u{pk(h0[0], h0[1]), pk(h0[2], h0[3]), pk(h1[0], h1[1]), pk(h1[2], h1[3])}, rws, h2_a);
        SCHED();
        STAMP(11);
        __builtin_amdgcn_s_barrier();
        W13_RANGE(NG + NSC, 2 * NB3);
        const f4 x0 = v0 + mean1, x1 = v1 + mean1;
        xo = v4u{pk(x0[0], x0[1]), pk(x0[2], x0[3]), pk(x1[0], x1[1]), pk(x1[2], x1[3])};
    }

    const v4u rs2 = mkrs4(pw2, 1792 * 512 * 2);
    const int P2 = L >> 4, rp = (L >> 3) & 1, ct = L & 7;
#define W2_DECL u32 v2a = (l >> 3) * 1024 + ct * 128 + ((((l >> 1) & 3) ^ ((l >> 4) & 1)) * 2 + (l & 1)) * 16, v2b = v2a ^ 64; \
        v4u w2f[28], hb[3][7]; u32 w2a[4], fl16
#define W2_ADDR() do { const int l_ = fresh_lane(); _Pragma("unroll") for (int nt = 0; nt < 4; ++nt) { \
        w2a[nt] = L_PARK + w * 12288 + (8 * (l_ >> 4) + ((l_ >> 2) & 3)) * 128 + (l_ & 3) * 8 + ((nt ^ ((l_ >> 3) & 3)) * 32); PIN(w2a[nt]); } } while (0)
#define W2_FRAG(i) w2f[i] = ldtr2(w2a[(i) & 3] + (((i) >> 2) % 3) * 4096, w2a[(i) & 3] + (((i) >> 2) % 3) * 4096 + 512)
    const u32 w2s = w * 229376, w2l = L_PARK + w * 12288;
#define W2_DMA(i) dma16c<(i) * 8192, ((i) % 12) * 1024, false>(rs2, ((i) & 1) ? v2b : v2a, w2s, w2l)
#define PW(w) ((w) & 3)
#define FL_ADDR(f) (WS_FL + P2 * 384 + (f) * 128 + (fresh_lane() & 15) * 4)
#define FL_LOAD(f) fl16 = __builtin_amdgcn_raw_buffer_load_b32(rws, FL_ADDR(f), 0, SC1)
#define FL_STORE(f) do { if (w == 0) __builtin_amdgcn_raw_buffer_store_b32(tag, rws, fresh_lane() == 0 ? WS_FL + P * 384 + (f) * 128 + mem * 4 : 0x7FFFFFF0u, 0, SC1); } while (0)
#define H_LOAD1(q, k) hb[(q) % 3][k] = ld128c(rws, h_rd + ((k) & 3) * 1024, (q) * 114688 + ((k) >> 2) * 4096)
#define H_LOAD(q) do { _Pragma("unroll") for (int k = 0; k < 7; ++k) H_LOAD1(q, k); } while (0)
#define G2_STEP(q, k) do { _Pragma("unroll") for (int nt = 0; nt < 4; ++nt) { \
        if ((k) == 0) mfma16z(o[nt], w2f[nt], hb[(q) % 3][0]); else mfma16a(o[nt], w2f[4 * (k) + nt], hb[(q) % 3][k]); } } while (0)

    auto gemm2 = [&](const v4u (&w2f)[28], v4u (&hb)[3][7], u32 h_rd, bool slow) __attribute__((always_inline)) {
        STAMP(13); STAMPC(62);
        const int l = fresh_lane(), li = l & 15, g = l >> 4;
        const int qq = w >> 1, cg = (w & 1) * 4 + g, pr = (2 * qq + rp) * 16 + li;
        const u32 xro = WS_XR + (P2 * 128 + pr) * 1024 + (4 * (2 * ct + (cg >> 2)) + 2 * (cg & 1)) * 16 + ((cg >> 1) & 1) * 8;
        v2u xa, xb; v4u g0, g1;
        u32 po, po2, oo, fl16; f4 x0, x1, b0, b1;
        FL_LOAD(2);
        xa = ld64c(rws, xro); xb = ld64c(rws, xro + 16);
        g0 = ld128c(rws, cg * 32, WS_G1 + 6144 + ct * 256); g1 = ld128c(rws, cg * 32 + 16, WS_G1 + 6144 + ct * 256);
        SCHED();
#pragma unroll
        for (int q = 0; q < 4; ++q) {
            if (q == 1) prio<2>(); if (q == 2) prio<1>(); if (q == 3) prio<0>();
            f4 o[4]; u32 mx = 0;
#pragma unroll
            for (int k = 0; k < 7; ++k) {
                G2_STEP(q, k);
                SCHED();
                if (q == 3) { mx = vmax(hb[0][k], mx); PIN(mx); }
                if (q == 1) { if (k < 4) { H_LOAD1(2, 2 * k); if (k < 3) H_LOAD1(2, 2 * k + 1); } else H_LOAD1(3, k - 4); }
                if (q == 2 && k < 4) H_LOAD1(3, k + 3);
                if (q == 0 && k == 2) {
                    po = (qq < 3 ? L_PARK + qq * 4096 : 0) + (cg >> 1) * 1024 + (cg & 1) * 512 + li * 16; po2 = po + (qq < 3 ? 49152 : 4096);
                    const int m2 = pr >> 3, L2 = (P2 >> 3) * 128 + (m2 >> 2) * 32 + (P2 & 7) * 4 + (m2 & 3);
                    oo = row_of(L2, pr & 7) * 1024 + ct * 128 + cg * 16;
                    PIN(oo); PIN(po); PIN(po2);
                }
                SCHED();
            }
            if (q == 0) RETRY(fl16 != tag, { if (slow) nap<8>(); FL_LOAD(2); });
            asm volatile("s_nop 7" : "+v"(o[0]), "+v"(o[1]), "+v"(o[2]), "+v"(o[3]), "+v"(mx));
            if (q == 3 && __builtin_expect(anylane(mx == SENT), 0)) {
                u32 sp_ = 0;
#pragma nounroll
                do { if (slow) nap<4>(); asm volatile("" ::: "memory"); H_LOAD(q); } while (anylane(vmaxn<7>(hb[q % 3]) == SENT) && ++sp_ < SPIN_CAP);
#pragma unroll
                for (int k = 0; k < 7; ++k) { asm volatile("s_nop 1" ::: "memory"); G2_STEP(q, k); }
                asm volatile("s_nop 7" : "+v"(o[0]), "+v"(o[1]), "+v"(o[2]), "+v"(o[3]));
            }
            STAMP(34 + q);
#pragma unroll
            for (int nt = 0; nt < 4; ++nt) lds<f4>((q < 3 ? L_PARK + q * 4096 : 0) + (q < 3 ? w : PW(w)) * 12288 + (q < 3 ? 0 : (w >> 2) * 4096) + nt * 1024 + l * 16) = o[nt];
            SCHED();
        }
        x0 = f4{lo16(xa.x), hi16(xa.x), lo16(xa.y), hi16(xa.y)}; x1 = f4{lo16(xb.x), hi16(xb.x), lo16(xb.y), hi16(xb.y)};
        b0 = f4{lo16(g0.x), hi16(g0.x), lo16(g0.z), hi16(g0.z)}; b1 = f4{lo16(g1.x), hi16(g1.x), lo16(g1.z), hi16(g1.z)};
        PIN(x0); PIN(x1); PIN(b0); PIN(b1);
        BAR();
        STAMP(14);
        f4 s0 = z4, s1 = z4;
#pragma unroll
        for (int i = 0; i < 8; ++i) { const u32 o_ = (i < 4 ? po : po2) + (i & 3) * 12288; s0 += lds<f4>(o_); s1 += lds<f4>(o_ + 256); }
        x0 += b0 * s0; x1 += b1 * s1;
        __builtin_amdgcn_raw_buffer_store_b128(v4u{pk(x0[0], x0[1]), pk(x0[2], x0[3]), pk(x1[0], x1[1]), pk(x1[2], x1[3])}, mkrs(pout, 2048 * 1024), oo, 0, 0);
        STAMP(15); STAMPC(63);
        STAMP_FLUSH();
    };

    if (!act) st128c(xo, rws, h2_w, WS_XR);
    int L_ = L; asm volatile("" : "+s"(L_));
    if (act) {
        const int l = fresh_lane();
        u32 a_v = (l >> 3) * 1024 + (((l & 7) ^ (4 * (w & 1) + (l >> 4))) * 16);
        u32 a_s = WS_H2 + (P * 128 + 8 * (w & 3)) * 1024 + (w >> 2) * 128, a_l = w * 1024;
#define A_DMA(n, h) dma16c<((n) >> 1) * 32768 + ((n) & 1) * 512 + (h) * 256, ((n) & 3) * SLOT + (h) * 8192, true>(rws4, a_v, a_s, a_l)
#define A_RD(n) do { ck0 = lds<v4u>(a_c + ((n) & 3) * SLOT); ck1 = lds<v4u>(a_c + ((n) & 3) * SLOT + 8192); } while (0)
#define A_CHK(n) RETRY(vmax(ck1, vmax(ck0, 0)) == SENT, { A_DMA(n, 0); A_DMA(n, 1); VM0(); A_RD(n); })
        W2_DECL;
        const int ft = 8 * cbk + w;
        u32 hh_w = WS_HH + (P * 8 + ((l >> 4) & 1)) * 57344 + (ft >> 1) * 1024 + ((ft & 1) * 2 + (l >> 5)) * 256 + (l & 15) * 16;
        u32 a_c = w * 1024 + l * 16;
        u32 b_r[4];
#pragma unroll
        for (int i = 0; i < 4; ++i) { b_r[i] = (l & 31) * 128 + (((2 * i + (l >> 5)) ^ ((l >> 1) & 7)) * 16); PIN(b_r[i]); }
        u32 h_rd = 0, xr_w = h2_w + WS_XR;
        v4u ck0, ck1;
        PIN(a_v); PIN(a_c); PIN(v2a); PIN(v2b); PIN(hh_w); PIN(xr_w); PIN(xo);
        asm volatile("" : "+s"(a_s), "+s"(a_l));
        SCHED();
        VM0();
        STAMP(43);
        nap<D_E3>();
        A_DMA(0, 0); A_DMA(0, 1);
        __builtin_amdgcn_s_barrier();
        sfor<3>([&](auto i) { A_DMA(i.value + 1, 0); A_DMA(i.value + 1, 1); });
        SCHED();
        W13_READ(NB1 + NB2, NB3);
        SCHED();
        waitvm<6>();
        A_RD(0); A_CHK(0);
        BAR();
        STAMP(12); STAMPC(61);
        v4u bf[4];
        f16v acc[2];
#define B_ONE(J, hf) bf[2 * ((J) & 1) + (hf)] = lds<v4u>(b_r[2 * ((J) & 1) + (hf)] + (((J) >> 3) & 3) * SLOT + (((J) & 7) >> 1) * 4096)
#define B_READ(J) do { B_ONE(J, 1); SCHED(); B_ONE(J, 0); } while (0)
#define HH_STORE(mt) do { PIN(hh_w); st128c(v4u{hp[0], hp[1], hp[2], hp[3]}, rws, hh_w, (mt) * 114688); } while (0)
#define GATE(mt, r) acc[(mt) & 1][r]
#define UPV(mt, r) acc[(mt) & 1][(r) + 8]
#define EP_A(mt, r) do { hv[(r) & 1] = __builtin_amdgcn_exp2f(-1.44269504089f * GATE(mt, r)); pv = GATE(mt, r) * UPV(mt, r); PIN(hv[(r) & 1]); PIN(pv); } while (0)
#define EP_B(mt, r) do { hv[(r) & 1] = pv * __builtin_amdgcn_rcpf(1.0f + hv[(r) & 1]); \
        if ((r) & 1) { hp[(r) >> 1] = pk(hv[0], hv[1]); PIN(hp[(r) >> 1]); } else PIN(hv[0]); } while (0)
        B_READ(0); B_READ(1);
        sfor<8>([&](auto nn) {
            constexpr int n = nn.value, mt = n >> 1, kh = n & 1;
            float hv[2], pv; u32 hp[4];
            sfor<16>([&](auto uu) {
                constexpr int u = uu.value, J = 8 * n + (u >> 1);
                if (u == 0) prio<2>(); if (u == 4) prio<1>(); if (u == 8) prio<0>();
                if (kh == 0 && u == 0) mfma32z(acc[mt & 1], wf[0], bf[0]); else mfma32a(acc[mt & 1], wf[16 * kh + u], bf[u & 3]);
                SCHED();
                if (u == 13) {
                    if (n == 3) waitvm<7>();
                    if (n == 5) { waitvm<0>(); PIN(fl16); RETRY(fl16 != tag, FL_LOAD(0)); }
                    if (n == 7) waitvm<5>();
                    BAR();
                    prio<3>();
                    STAMP(17 + 2 * n);
                }
                if ((u & 1) && J + 2 < 64) B_READ(J + 2);
                if (u == 13 && (n == 3 || n == 5 || n == 7)) FL_STORE((n - 3) >> 1);
                if (u == 5 && n < 7) {
                    if (n == 0) waitvm<4 + 1>(); if (n == 1) waitvm<2 + 6 + 3>(); if (n == 2) waitvm<3>(); if (n == 3) waitvm<6 + 6 + 3>();
                    if (n == 4) waitvm<4 + 8 + 3>(); if (n == 5) waitvm<6 + 7 + 2>(); if (n == 6) waitvm<5 + 2 + 1>();
                    A_RD(n + 1);
                }
                if (u == 9 && n < 7) A_CHK(n + 1);
                if constexpr (n >= 1 && n <= 4 && (u == 0 || u == 2)) A_DMA(n + 3, u >> 1);
                if constexpr (n < 2 && !(u & 1) && u >= 4) W2_DMA(6 * n + ((u - 4) >> 1));
                if constexpr (n == 2 && (u == 12 || u == 14)) W2_DMA(6 + (u >> 1));
                if constexpr (n == 3 && !(u & 1) && u >= 4) W2_DMA(14 + ((u - 4) >> 1));
                if constexpr (n == 4 && !(u & 1) && u >= 4 && u < 12) W2_DMA(20 + ((u - 4) >> 1));
                if constexpr (n == 6 && (u & 1) && u >= 9) W2_DMA(24 + ((u - 9) >> 1));
                if (n == 2 && u == 4) st128c(xo, rws, xr_w);
                if (n == 5 && u == 0) { PIN(hh_w); st128c(sent4, rws, hh_w, 3 * 114688); }
                if (n == 2 && u == 1) { W2_ADDR(); waitvm<8 + 1 + 2>(); }
                if constexpr (n == 2 && u >= 2 && u < 14) W2_FRAG(u - 2);
                if (n == 5 && u == 2) ld32a(fl16, rws4, FL_ADDR(0));
                if (n == 6 && u == 1) { h_rd = WS_HH + (P2 * 8 + rp) * 57344 + w * 7168 + fresh_lane() * 16; PIN(h_rd); }
                if constexpr (n == 6 && u >= 1 && u < 5) W2_FRAG(11 + u);
                if (n == 6 && u == 13) ld32a(fl16, rws4, FL_ADDR(1));
                if constexpr (n == 6 && u >= 4 && !(u & 1)) H_LOAD1(0, (u - 4) >> 1);
                if constexpr (n == 6 && u == 15) H_LOAD1(0, 6);
                if (n == 2 || n == 4 || n == 6) {
                    if (u == 1) asm volatile("s_nop 3" : "+v"(acc[(mt - 1) & 1]));
                    if (u == 1) { EP_A(mt - 1, 0); EP_B(mt - 1, 0); }
                    if (u >= 2) { if (u & 1) EP_B(mt - 1, u >> 1); else EP_A(mt - 1, u >> 1); }
                    if (u == 15) HH_STORE(mt - 1);
                }
                if constexpr (n == 7 && u < 8) W2_FRAG(16 + u);
                if (n == 7 && u == 7) { waitvm<4>(); PIN(fl16); RETRY(fl16 != tag, FL_LOAD(1)); }
                if constexpr (n == 7 && u >= 8 && u < 15) H_LOAD1(1, u - 8);
                if constexpr (n == 7 && u >= 14) { W2_FRAG(2 * u - 4); W2_FRAG(2 * u - 3); }
                SCHED();
            });
        });
        STAMP(42); STAMPC(59);
        {
            asm volatile("s_nop 7" : "+v"(acc[1]));
            u32 hp[4];
#pragma unroll
            for (int r = 0; r < 4; ++r) hp[r] = pk(silu(GATE(3, 2 * r)) * UPV(3, 2 * r), silu(GATE(3, 2 * r + 1)) * UPV(3, 2 * r + 1));
            HH_STORE(3);
        }
        SCHED();
        __builtin_amdgcn_s_barrier();
        gemm2(w2f, hb, h_rd, false);
    }
    if (!((L_ & 3) < 3 || (((L_ >> 5) ^ (L_ >> 2)) & 1))) {
        const int l = fresh_lane();
        W2_DECL;
        W2_ADDR();
        VM0();
        __builtin_amdgcn_s_barrier();
        sfor<3>([&](auto r) {
            sfor<12>([&](auto i) { if constexpr (12 * r.value + i.value < 28) W2_DMA(12 * r.value + i.value); });
            VM0();
            if (r.value == 0) { BAR(); FL_STORE(0); FL_STORE(1); FL_STORE(2); }
            sfor<12>([&](auto i) { if constexpr (12 * r.value + i.value < 28) W2_FRAG(12 * r.value + i.value); });
            asm volatile("s_waitcnt lgkmcnt(0)" ::: "memory");
        });
        nap<D_IDLE>();
        FL_LOAD(1);
        RETRY(fl16 != tag, { nap<8>(); FL_LOAD(1); });
        const u32 h_rd = WS_HH + (P2 * 8 + rp) * 57344 + w * 7168 + l * 16;
        H_LOAD(0); H_LOAD(1);
        nap<24>();
        gemm2(w2f, hb, h_rd, true);
        asm volatile(".Lcode_tail:");
    }
}

extern "C" void kernel_launch(void* const* d_in, const int* in_sizes, int n_in, void* d_out, int out_size, void* d_ws, size_t ws_size,
                              hipStream_t stream) {
    (void)in_sizes; (void)n_in; (void)out_size; (void)ws_size;
    const Args a = {(const u16*)d_in[8], (const u16*)d_in[7], (unsigned char*)d_ws, (const u16*)d_in[0], (const u16*)d_in[1],
                    (const u16*)d_in[2], (const u16*)d_in[4], (const u16*)d_in[5], (const u16*)d_in[6], (u16*)d_out};
    fused_kernel<<<256, 512, 0, stream>>>(a);
}
```

```cpp
#include <hip/hip_runtime.h>
#include <hip/hip_bf16.h>
#include <type_traits>

typedef unsigned u32;
typedef unsigned short u16;
typedef u32 v4u __attribute__((ext_vector_type(4)));
typedef u32 v2u __attribute__((ext_vector_type(2)));
typedef float f2 __attribute__((ext_vector_type(2)));
typedef float f4 __attribute__((ext_vector_type(4)));
typedef float f16v __attribute__((ext_vector_type(16)));
typedef short s4 __attribute__((ext_vector_type(4)));
typedef __bf16 b8 __attribute__((ext_vector_type(8)));
typedef __bf16 b2 __attribute__((ext_vector_type(2)));
typedef __amdgpu_buffer_rsrc_t rsrc_t;
#define LAS __attribute__((address_space(3)))
#define DEV __device__ __forceinline__

constexpr u32 WS_EPOCH = 0;
constexpr u32 WS_TK = 16384;
constexpr u32 WS_TKB = 24576;
constexpr u32 WS_FL = 32768;
constexpr u32 WS_G0 = 65536;
constexpr u32 WS_G1 = 8192;
constexpr u32 XSTR = 8320;
constexpr u32 WS_WB = 40960;
constexpr u32 WS_LP = 131072;
constexpr u32 WS_X1 = 262144;
constexpr u32 WS_WT = WS_X1 + 256 * XSTR;
constexpr u32 WS_X2 = WS_WT + 256 * XSTR;
constexpr u32 WS_H2 = WS_X2 + 256 * XSTR;
constexpr u32 WS_XR = WS_H2 + 2048 * 1024;
constexpr u32 WS_HH = WS_XR + 2048 * 1024;
constexpr u32 WS_END = WS_HH + 2048 * 1792 * 2;
constexpr u32 WS_DBG = WS_END;

constexpr u32 SLOT = 16384;
constexpr u32 L_PARK = 65536;
constexpr u32 L_XS = 0;
constexpr u32 L_T0 = 8192;
constexpr u32 L_T1 = L_T0 + 8320;
constexpr u32 WRS = 2064;
#define WRB(v) ((v) * WRS + ((v) >> 2) * 32)
constexpr u32 L_WR = L_T0;
constexpr u32 L_MF = L_XS;
constexpr u32 L_E1 = L_XS;
constexpr u32 L_E2 = L_T1 + 8320;
constexpr u32 L_MOD0 = 33792;
constexpr u32 L_MOD1 = L_MOD0 + 2048;
constexpr u32 L_TAB = 40960;
constexpr u32 L_SCRAP = L_TAB + 16384;
constexpr u32 L_LT = L_E2;
constexpr u32 L_BIG = 65536;
constexpr u32 L_TOTAL = 163840;
static_assert(L_E2 + 8 * 1088 <= L_MOD0 && L_MOD1 + 4096 <= L_BIG && L_TOTAL <= 163840, "lds");

constexpr u32 SENT = 0xFFFFFFFFu;
constexpr u32 SPIN_CAP = 1u << 16;
constexpr int SC1 = 16;
#define NT 0
#define D_E0 2
#define D_E1 0
#define D_E2 0
#define D_IDLE 12
#define D_E3 0
#define NB1 4
#define NB2 6
#define NB3 6
static_assert(NB1 + NB2 + NB3 == 16 && NB1 <= 6 && NB2 <= 6 && NB3 <= 6 , "schedule");
#define F2P 1
#define NSB 0
#define NSC 0
#define KS 4
#define WARMC 1
#define CBP 0
#define ROT 0
#define LEAD 1
#define W2W 1
#define FLK 6
#define FL1U 11
#define XQ 0
#define TP 3
#define NH 6
#define D_I2 16
#define NIP 8
#define WX 40
#define C_IDLE 13500
#define D_IW 0
#define IM 7
#define ABL 0
#define MB 0
#define MC 0
#define TLBW 0
#define SH16 1
#define LPP 0
#define D_I1 6
#define EPK 0
#define FEWBAR 1
#define EPN 15
#define EP0 1
#define NBP 2
#define FLH 0
#define LTK 0
#define NK 2
#define G0B 0
#define TOUCH 2
constexpr int GW = 1;
#define N1 0
#define NA 0
#define ND 0
#define NG 0
#define D_TK 0
constexpr u32 OOB = 0x7FFF0000u;

constexpr double c_sin(int num, int den) {
    num = ((num % den) + den) % den;
    double sgn = 1.0;
    if (2 * num >= den) { num -= den / 2; sgn = -1.0; }
    if (4 * num > den) num = den / 2 - num;
    const double x = 6.283185307179586476925 * num / den;
    double term = x, sum = x;
    for (int i = 1; i < 14; ++i) { term *= -x * x / ((2 * i) * (2 * i + 1)); sum += term; }
    return sgn * sum;
}
constexpr double c_cos(int num, int den) { return c_sin(4 * num + den, 4 * den); }
constexpr u32 c_f32(double v) { return __builtin_bit_cast(u32, (float)v); }
constexpr u32 c_bf(double v) { const u32 u = c_f32(v + 0.0); return ((u + 0x7FFFu + ((u >> 16) & 1u)) >> 16) & 0xFFFFu; }
constexpr u32 c_pk(double a, double b) { return c_bf(a) | (c_bf(b) << 16); }
enum { T_aBR, T_aBI, T_FR, T_aAI, T_aSR, T_aSI, T_tc, T_ts, T_FI, T_A8, T_cBR, T_cBI, T_cSR, T_cSI, T_cB0, T_cB1, T_N };
struct Tab { u32 v[T_N * 256]; };
constexpr Tab make_tab() {
    Tab T{};
    for (int l = 0; l < 64; ++l)
        for (int r = 0; r < 4; ++r) {
            const int li = l & 15, g = l >> 4, n = 4 * g + r, o = l * 4 + r;
            const int n0 = 4 * g + 16 * (r >> 1) + 2 * (r & 1);
            T.v[T_aBR * 256 + o] = c_pk(c_cos(n0 * li, 32), c_cos((n0 + 1) * li, 32));
            T.v[T_aBI * 256 + o] = li ? c_pk(-c_sin(n0 * li, 32), -c_sin((n0 + 1) * li, 32)) : c_pk(1, -1);
            const double c = c_cos(n * li, 16), s = c_sin(n * li, 16);
            T.v[T_FR * 256 + o] = c_pk(c, s);
            T.v[T_FI * 256 + o] = c_pk(-s, c);
            T.v[T_aAI * 256 + o] = li < 8 ? c_pk(-s, c) : c_pk(s, -c);
            T.v[T_aSR * 256 + o] = li < 8 ? c_pk(c, 0) : c_pk(0, c_cos(n * (2 * li - 15), 32));
            T.v[T_aSI * 256 + o] = li == 0 ? c_pk((r & 1) ? -1 : 1, 0) : li < 8 ? c_pk(-s, 0) : c_pk(0, -c_sin(n * (2 * li - 15), 32));
            T.v[T_tc * 256 + o] = c_f32(c_cos(n * li, 512));
            T.v[T_ts * 256 + o] = c_f32(c_sin(n * li, 512));
            const int b = 2 * g + (r & 1), a8 = b * ((li >> 1) + (r >> 1)) + 2 * (li & 1);
            T.v[T_A8 * 256 + o] = c_pk(c_cos(a8, 8), (r >> 1) ? -c_sin(a8, 8) : c_sin(a8, 8));
            const double sg = g < 2 ? 1 : -1;
            T.v[T_cBR * 256 + o] = c_pk(c, -sg * s);
            T.v[T_cBI * 256 + o] = c_pk(s, sg * c);
            T.v[T_cSR * 256 + o] = n == 0 ? c_pk(1, (li & 1) ? -1 : 1) : g < 2 ? c_pk(2 * c, -2 * s) : 0u;
            T.v[T_cSI * 256 + o] = g < 2 ? 0u : c_pk(2 * c_cos((2 * n - 15) * li, 32), -2 * c_sin((2 * n - 15) * li, 32));
            const double k = 1.0 / 1048576;
            T.v[T_cB0 * 256 + o] = n == 0 ? c_pk(k, k) : c_pk(2 * k * c, -2 * k * s);
            T.v[T_cB1 * 256 + o] = n == 0 ? c_pk(k, -k) : c_pk(2 * k * c_cos((2 * li + 1) * n, 32), -2 * k * c_sin((2 * li + 1) * n, 32));
        }
    return T;
}
__device__ __attribute__((aligned(128))) const Tab g_tab = make_tab();

__shared__ __attribute__((aligned(16))) unsigned char smem[L_TOTAL];

DEV u32 pk(float lo, float hi) { f2 v = {lo, hi}; return __builtin_bit_cast(u32, __builtin_convertvector(v, b2)); }
DEV float lo16(u32 x) { return __builtin_bit_cast(float, x << 16); }
DEV float hi16(u32 x) { return __builtin_bit_cast(float, x & 0xFFFF0000u); }
DEV float bfr(float x) { return lo16(pk(x, x)); }
DEV float cosr(float rev) { return __builtin_amdgcn_cosf(rev); }
DEV float sinr(float rev) { return __builtin_amdgcn_sinf(rev); }
DEV f4 mfma16(v4u a, v4u b, f4 c) { return __builtin_amdgcn_mfma_f32_16x16x32_bf16(__builtin_bit_cast(b8, a), __builtin_bit_cast(b8, b), c, 0, 0, 0); }
DEV void mfma16a(f4& c, v4u a, v4u b) { asm volatile("v_mfma_f32_16x16x32_bf16 %0, %1, %2, %0" : "+v"(c) : "v"(a), "v"(b)); }
DEV void mfma16z(f4& c, v4u a, v4u b) { asm volatile("v_mfma_f32_16x16x32_bf16 %0, %1, %2, 0" : "=&v"(c) : "v"(a), "v"(b)); }
DEV void mfma32a(f16v& c, v4u a, v4u b) { asm volatile("v_mfma_f32_32x32x16_bf16 %0, %1, %2, %0" : "+v"(c) : "v"(a), "v"(b)); }
DEV void mfma32z(f16v& c, v4u a, v4u b) { asm volatile("v_mfma_f32_32x32x16_bf16 %0, %1, %2, 0" : "=&v"(c) : "v"(a), "v"(b)); }
template <bool V> DEV void mf2(f4& d0, f4& d1, v4u a0, v4u b0, v4u a1, v4u b1) {
    if (V) asm volatile("s_nop 1\n\tv_mfma_f32_16x16x32_bf16 %0, %2, %3, 0\n\tv_mfma_f32_16x16x32_bf16 %1, %4, %5, 0" : "=&v"(d0), "=&v"(d1) : "v"(a0), "v"(b0), "v"(a1), "v"(b1));
    else asm volatile("v_mfma_f32_16x16x32_bf16 %0, %2, %3, 0\n\tv_mfma_f32_16x16x32_bf16 %1, %4, %5, 0" : "=&v"(d0), "=&v"(d1) : "v"(a0), "v"(b0), "v"(a1), "v"(b1));
}
DEV void mf2acc(f4& d0, f4& d1, v4u a, v4u b0, v4u b1) {
    asm volatile("s_nop 1\n\tv_mfma_f32_16x16x32_bf16 %0, %2, %3, %0\n\tv_mfma_f32_16x16x32_bf16 %1, %2, %4, %1" : "+v"(d0), "+v"(d1) : "v"(a), "v"(b0), "v"(b1));
}
DEV void mf2k(f4& d0, f4& d1, v2u a, v2u b0, v2u b1) {
    asm volatile("s_nop 1\n\tv_mfma_f32_16x16x16_bf16 %0, %2, %3, 0\n\tv_mfma_f32_16x16x16_bf16 %1, %2, %4, 0" : "=&v"(d0), "=&v"(d1) : "v"(a), "v"(b0), "v"(b1));
}
template <int N> DEV void gap(f4& d0, f4& d1) { asm volatile("s_nop %2" : "+v"(d0), "+v"(d1) : "n"(N - 1)); }
DEV f4 mfma16k(v2u a, v2u b, f4 c) { return __builtin_amdgcn_mfma_f32_16x16x16bf16_1k(__builtin_bit_cast(s4, a), __builtin_bit_cast(s4, b), c, 0, 0, 0); }
DEV f16v mfma32(v4u a, v4u b, f16v c) { return __builtin_amdgcn_mfma_f32_32x32x16_bf16(__builtin_bit_cast(b8, a), __builtin_bit_cast(b8, b), c, 0, 0, 0); }
DEV v2u ldtr(u32 off) { return __builtin_bit_cast(v2u, __builtin_amdgcn_ds_read_tr16_b64_v4i16((LAS s4*)(smem + off))); }
DEV v4u ldtr2(u32 off0, u32 off1) { v2u a = ldtr(off0), b = ldtr(off1); return v4u{a.x, a.y, b.x, b.y}; }
template <class T> DEV T& lds(u32 off) { return *(T*)(smem + off); }
DEV rsrc_t mkrs(const void* p, u32 bytes) { return __builtin_amdgcn_make_buffer_rsrc((void*)p, 0, bytes, 0x00020000); }
DEV v4u mkrs4(const void* p, u32 bytes) { unsigned long long a = (unsigned long long)p; return v4u{(u32)a, (u32)(a >> 32) & 0xFFFFu, bytes, 0x00020000u}; }
DEV v4u ld128(rsrc_t r, u32 off) { return __builtin_amdgcn_raw_buffer_load_b128(r, off, 0, 0); }
DEV v4u ld128n(rsrc_t r, u32 off, u32 soff = 0) { return __builtin_amdgcn_raw_buffer_load_b128(r, off, soff, NT); }
DEV v2u ld64n(rsrc_t r, u32 off, u32 soff = 0) { return __builtin_amdgcn_raw_buffer_load_b64(r, off, soff, NT); }
DEV v2u ld64(rsrc_t r, u32 off) { return __builtin_amdgcn_raw_buffer_load_b64(r, off, 0, 0); }
DEV u32 ld16(rsrc_t r, u32 off) { return (u32)__builtin_amdgcn_raw_buffer_load_b16(r, off, 0, 0); }
DEV v4u ld128c(rsrc_t r, u32 off, u32 soff = 0) { return __builtin_amdgcn_raw_buffer_load_b128(r, off, soff, SC1); }
DEV v2u ld64c(rsrc_t r, u32 off) { return __builtin_amdgcn_raw_buffer_load_b64(r, off, 0, SC1); }
DEV void st128c(v4u v, rsrc_t r, u32 off, u32 base = 0) { __builtin_amdgcn_raw_buffer_store_b128(v, r, off + base, 0, SC1); }
DEV void dma16(v4u rs, u32 voff, u32 soff, u32 ldsa) {
    asm volatile("s_mov_b32 m0, %3\n\ts_nop 0\n\tbuffer_load_dwordx4 %0, %1, %2 offen lds" ::"v"(voff), "s"(rs), "s"(soff), "s"(ldsa) : "memory");
}
template <int SOFF, int LOFF, bool COH> DEV void dma16c(v4u rs, u32 voff, u32 sbase, u32 lbase) {
    u32 tmp;
    if (COH) asm volatile("s_add_u32 m0, %4, %6\n\ts_add_u32 %0, %3, %5\n\tbuffer_load_dwordx4 %1, %2, %0 offen sc1 lds" : "=&s"(tmp) : "v"(voff), "s"(rs), "s"(sbase), "s"(lbase), "n"(SOFF), "n"(LOFF) : "memory", "scc");
    else asm volatile("s_add_u32 m0, %4, %6\n\ts_add_u32 %0, %3, %5\n\tbuffer_load_dwordx4 %1, %2, %0 offen lds" : "=&s"(tmp) : "v"(voff), "s"(rs), "s"(sbase), "s"(lbase), "n"(SOFF), "n"(LOFF) : "memory", "scc");
}
template <int LOFF> DEV void dma16d(v4u rs, u32 voff, u32 soff, u32 lbase) {
    asm volatile("s_add_u32 m0, %3, %4\n\ts_nop 0\n\tbuffer_load_dwordx4 %0, %1, %2 offen lds" ::"v"(voff), "s"(rs), "s"(soff), "s"(lbase), "n"(LOFF) : "memory", "scc");
}
#define NTS ""
#define NTR ""
DEV void dma16n(v4u rs, u32 voff, u32 soff, u32 ldsa) {
    asm volatile("s_mov_b32 m0, %3\n\ts_nop 0\n\tbuffer_load_dwordx4 %0, %1, %2 offen " NTR " lds" ::"v"(voff), "s"(rs), "s"(soff), "s"(ldsa) : "memory");
}
#define NTT ""
DEV void dma16t(v4u rs, u32 voff, u32 soff, u32 ldsa) {
    asm volatile("s_mov_b32 m0, %3\n\ts_nop 0\n\tbuffer_load_dwordx4 %0, %1, %2 offen " NTT " lds" ::"v"(voff), "s"(rs), "s"(soff), "s"(ldsa) : "memory");
}
DEV void dma4s(v4u rs, u32 voff, u32 soff, u32 ldsa) {
    asm volatile("s_mov_b32 m0, %2\n\ts_nop 0\n\tbuffer_load_dword %0, %1, %3 offen lds" ::"v"(voff), "s"(rs), "s"(ldsa), "s"(soff) : "memory");
}
DEV void dma4(v4u rs, u32 voff, u32 ldsa) {
    asm volatile("s_mov_b32 m0, %2\n\ts_nop 0\n\tbuffer_load_dword %0, %1, 0 offen lds" ::"v"(voff), "s"(rs), "s"(ldsa) : "memory");
}
template <int N, class F> DEV void sfor(F&& f) { if constexpr (N > 0) { sfor<N - 1>(f); f(std::integral_constant<int, N - 1>{}); } }
DEV void ld128a(v4u& d, v4u rs, u32 voff) { asm volatile("buffer_load_dwordx4 %0, %1, %2, 0 offen sc1" : "=v"(d) : "v"(voff), "s"(rs) : "memory"); }
DEV void ld128a(v4u& d, v4u rs, u32 voff, u32 soff) { asm volatile("buffer_load_dwordx4 %0, %1, %2, %3 offen sc1" : "=v"(d) : "v"(voff), "s"(rs), "s"(soff) : "memory"); }
template <int N, class T> DEV void waitv(T& d) { asm volatile("s_waitcnt vmcnt(%1)" : "+v"(d) : "n"(N) : "memory"); }
#define CW(a, b) (a)
template <int N> DEV void waitvm() { asm volatile("s_waitcnt vmcnt(%0)" ::"n"(N) : "memory"); }
DEV void ld64p(v2u& d, v4u rs, u32 voff, u32 soff) { asm volatile("buffer_load_dwordx2 %0, %1, %2, %3 offen " NTS : "=v"(d) : "v"(voff), "s"(rs), "s"(soff) : "memory"); }
DEV void ld128p(v4u& d, v4u rs, u32 voff, u32 soff) { asm volatile("buffer_load_dwordx4 %0, %1, %2, %3 offen" : "=v"(d) : "v"(voff), "s"(rs), "s"(soff) : "memory"); }
DEV void ld128p(v4u& d, v4u rs, u32 voff) { asm volatile("buffer_load_dwordx4 %0, %1, %2, 0 offen" : "=v"(d) : "v"(voff), "s"(rs) : "memory"); }
DEV void ld32p(u32& d, v4u rs, u32 voff) { asm volatile("buffer_load_dword %0, %1, %2, 0 offen" : "=v"(d) : "v"(voff), "s"(rs) : "memory"); }
DEV void reload128(v4u& d, v4u rs, u32 voff, u32 soff) { asm volatile("buffer_load_dwordx4 %0, %1, %2, %3 offen sc1\n\ts_waitcnt vmcnt(0)" : "+v"(d) : "v"(voff), "s"(rs), "s"(soff) : "memory"); }
DEV void ld64a(v2u& d, v4u rs, u32 voff) { asm volatile("buffer_load_dwordx2 %0, %1, %2, 0 offen sc1" : "=v"(d) : "v"(voff), "s"(rs) : "memory"); }
DEV void ld32a(u32& d, v4u rs, u32 voff) { asm volatile("buffer_load_dword %0, %1, %2, 0 offen sc1" : "=v"(d) : "v"(voff), "s"(rs) : "memory"); }
template <int N> DEV void prio() { __builtin_amdgcn_s_setprio(N); }
#define VM0() asm volatile("s_waitcnt vmcnt(0)" ::: "memory")
#define BAR() do { asm volatile("s_waitcnt lgkmcnt(0)" ::: "memory"); __builtin_amdgcn_s_barrier(); asm volatile("" ::: "memory"); } while (0)
#define SCHED() __builtin_amdgcn_sched_barrier(0)
#define PIN(x) asm volatile("" : "+v"(x))
#define RETRY(BAD, LOAD) do { if (__builtin_expect(anylane(BAD), 0)) { u32 sp_ = 0; \
        _Pragma("nounroll") do { asm volatile("" ::: "memory"); LOAD; } while (anylane(BAD) && ++sp_ < SPIN_CAP); } } while (0)
template <int N> DEV void nap() { if (N > 0) __builtin_amdgcn_s_sleep(N); }
DEV u32 umax3(u32 a, u32 b, u32 c) { return max(max(a, b), c); }
DEV u32 vmax(v4u v, u32 m) { return umax3(umax3(m, v.x, v.y), v.z, v.w); }
template <int N> DEV u32 vmaxn(const v4u (&v)[N]) { u32 m = 0;
#pragma unroll
    for (int i = 0; i < N; ++i) m = vmax(v[i], m);
    return m; }
DEV int fresh_lane() { int x; asm volatile("v_mbcnt_lo_u32_b32 %0, -1, 0\n\tv_mbcnt_hi_u32_b32 %0, -1, %0" : "=v"(x)); return x; }
DEV bool anylane(bool b) { return __builtin_amdgcn_ballot_w64(b) != 0ull; }

template <int CTRL, int RM> DEV float dppadd(float v) {
    return v + __builtin_bit_cast(float, __builtin_amdgcn_update_dpp(0, __builtin_bit_cast(int, v), CTRL, RM, 0xF, true));
}
template <int CTRL> DEV float dppget(float v) { return __builtin_bit_cast(float, __builtin_amdgcn_update_dpp(0, __builtin_bit_cast(int, v), CTRL, 0xF, 0xF, true)); }
DEV float swapadd16(float v) { const u32 x = __builtin_bit_cast(u32, v); const auto r = __builtin_amdgcn_permlane16_swap(x, x, false, false); const u32 a = r[0], b = r[1]; return __builtin_bit_cast(float, a) + __builtin_bit_cast(float, b); }
DEV float swapadd32(float v) { const u32 x = __builtin_bit_cast(u32, v); const auto r = __builtin_amdgcn_permlane32_swap(x, x, false, false); const u32 a = r[0], b = r[1]; return __builtin_bit_cast(float, a) + __builtin_bit_cast(float, b); }
DEV float tadd16(float a, float b) { const auto r = __builtin_amdgcn_permlane16_swap(__builtin_bit_cast(u32, a), __builtin_bit_cast(u32, b), false, false); const u32 x = r[0], y = r[1]; return __builtin_bit_cast(float, x) + __builtin_bit_cast(float, y); }
DEV float tadd32(float a, float b) { const auto r = __builtin_amdgcn_permlane32_swap(__builtin_bit_cast(u32, a), __builtin_bit_cast(u32, b), false, false); const u32 x = r[0], y = r[1]; return __builtin_bit_cast(float, x) + __builtin_bit_cast(float, y); }
DEV float wave_sum(float v) {
    v = dppadd<0xB1, 0xF>(v); v = dppadd<0x4E, 0xF>(v); v = dppadd<0x141, 0xF>(v); v = dppadd<0x140, 0xF>(v);
    v = dppadd<0x142, 0xF>(v); v = dppadd<0x143, 0xF>(v);
    return __builtin_bit_cast(float, __builtin_amdgcn_readlane(__builtin_bit_cast(int, v), 63));
}
DEV f2 wave_sum2(float a, float b) {
#define DPP2(C) do { a = dppadd<C, 0xF>(a); b = dppadd<C, 0xF>(b); SCHED(); } while (0)
    SCHED(); DPP2(0xB1); DPP2(0x4E); DPP2(0x141); DPP2(0x140); DPP2(0x142); DPP2(0x143);
    return f2{__builtin_bit_cast(float, __builtin_amdgcn_readlane(__builtin_bit_cast(int, a), 63)), __builtin_bit_cast(float, __builtin_amdgcn_readlane(__builtin_bit_cast(int, b), 63))};
}
DEV f2 cmul(f2 z, f2 w) {
    f2 r;
    asm("v_pk_mul_f32 %0, %1, %2 op_sel_hi:[1,0]\n\tv_pk_fma_f32 %0, %1, %2, %0 op_sel:[1,1,0] op_sel_hi:[0,1,1] neg_lo:[0,1,0]" : "=&v"(r) : "v"(z), "v"(w));
    return r;
}
DEV float silu(float x) { return x * __builtin_amdgcn_rcpf(1.0f + __builtin_amdgcn_exp2f(-1.44269504089f * x)); }

DEV void fwd_pair(float rev0, float drev, v4u& R, v4u& I) {
#pragma unroll
    for (int r = 0; r < 4; ++r) { const float a = rev0 + r * drev, c = cosr(a), s = sinr(a); R[r] = pk(c, s); I[r] = pk(-s, c); }
}
DEV v4u pk4(f4 a, f4 b) { return v4u{pk(a[0], b[0]), pk(a[1], b[1]), pk(a[2], b[2]), pk(a[3], b[3])}; }
DEV v4u twid_pack(f4 re, f4 im, const f4& c, const f4& s) {
    f4 a = re * c + im * s, b = im * c - re * s;
    PIN(a); PIN(b);
    return pk4(a, b);
}
DEV int panel_of(int L) { return (L >> 7) * 8 + (((L >> 2) & 7) ^ (((L >> 5) & 1) * (IM >> 2))); }
DEV int row_of(int L, int rho) { return 128 * (4 * ((L >> 3) & 3) + (rho & 3)) + 8 * (2 * (L & 7) + (rho >> 2)) + (L >> 5); }

#define STAMP(i)
#define STAMPC(i)
#define STAMP_FLUSH()

enum { OP_NONE = -1, OP_D = 0, OP_A = 100, OP_XR = 200, OP_FILL = 201, OP_HH = 210, OP_FL = 220, OP_H = 230 };
#define SU 15
constexpr int g1op(int T) {
    const int n = T >> 4, u = T & 15;
    if (u == SU) { if (n == 1) return OP_XR; if (n == 3) return OP_FILL; }
    if (T >= 32 && (T & 31) == EPN) return OP_HH + T / 32 - 1;
    if (n == 7 && u == FL1U) return OP_FL + 1;
    if (u & 1) return OP_NONE;
    if (n == 0 && FLH) return u == 0 ? OP_FL + 3 : u < 8 ? OP_D + u / 2 - 1 : OP_A + u / 2;
    if (n == 0) return u < 2 ? OP_NONE : u < 10 ? OP_A + 3 + u / 2 : OP_D + u / 2 - 5;
    if (n == 1) return u < 14 ? OP_D + 3 + u / 2 : OP_NONE;
    if (n == 2) return u < 2 ? OP_NONE : u < 6 ? OP_D + 9 + u / 2 : u < 14 ? OP_A + 5 + u / 2 : OP_NONE;
    if (n == 3) return u == 10 || u == 12 ? OP_D + 7 + u / 2 : OP_NONE;
    if (n == 4) return u < 2 ? OP_NONE : u < 10 ? OP_A + 11 + u / 2 : u < 14 ? OP_D + 9 + u / 2 : OP_NONE;
    if (n == 5) return OP_D + 16 + u / 2;
    if (n == 6) return u == 2 ? OP_FL : u >= 6 && u < 14 ? OP_D + 21 + u / 2 : OP_NONE;
    return u >= 2 ? OP_H + u / 2 - 1 : OP_NONE;
}
constexpr int g1after(int op, int T) { int c = 0; for (int i = 0; i < T; ++i) { if (g1op(i) == op) c = 0; else if (g1op(i) != OP_NONE) ++c; } return c; }
constexpr int g1pos(int op) { for (int i = 0; i < 128; ++i) if (g1op(i) == op) return i; return -1; }

struct Args { const u16 *pwm, *pc; unsigned char* ws; const u16 *px, *pwr, *pwi, *pw1, *pw3, *pw2; u16* pout; };
typedef u32 s16u __attribute__((ext_vector_type(16)));
DEV unsigned char* mkptr(u32 lo, u32 hi) { return (unsigned char*)(((unsigned long long)hi << 32) | lo); }

__global__ void __launch_bounds__(512, 2) fused_kernel(Args) {
    s16u ka; v4u kb4;
    asm volatile("s_load_dwordx16 %0, %2, 0x0\n\ts_load_dwordx4 %1, %2, 0x40" : "=&s"(ka), "=&s"(kb4) : "s"(__builtin_amdgcn_kernarg_segment_ptr()));
    const int t = threadIdx.x, l = t & 63, li = l & 15, g = l >> 4;
    const int w = __builtin_amdgcn_readfirstlane(t >> 6);
    const int bid = blockIdx.x, L = (bid & 7) * 32 + (bid >> 3);
    if (w == 0) {
        unsigned long long pcv; u32 len;
        asm volatile("s_getpc_b64 %0\n.Lpcref%=:\n\ts_mov_b32 %1, .Lcode_tail-.Lpcref%=" : "=s"(pcv), "=s"(len));
        dma16(mkrs4((const void*)pcv, len), l < 12 ? (l * 32 + (L & 31)) * 128 : OOB, 0, L_SCRAP);
    }
    const v4u rt = mkrs4(&g_tab, sizeof(Tab));
    if (w == 2) dma4(rt, l < 4 ? (l * 32 + (L & 31)) * 128 : OOB, L_SCRAP);
    const int kapB = L ^ 3;
    u32 x1_w = WS_X1 + L * XSTR + t * 16, x2_w = WS_X2 + kapB * XSTR + t * 16, e0a;
    constexpr int LPR = KS / 2, RPI = 64 / LPR, SPX = 32 / KS;
    const int kq = (L & 31) / SPX, gcol = (L >> 5) * 128 + (L & (SPX - 1)) * 8 * LPR;
    e0a = (kq * (512 / KS) + l / LPR) * 6144 + (gcol + (l % LPR) * 8) * 2;
    const v4u sent4 = {SENT, SENT, SENT, SENT};
    u32 wro, xso = row_of(L, w) * 1024, wla = L_WR + WRB(w);
    {
        const int tt = 2 * L + (w >> 2), j = w & 3;
        const int pos = 32 * (tt >> 6) + 2 * (tt & 15) + (j >> 1), kb = 2 * ((tt >> 4) & 3) + (j & 1);
        const int k = ((pos >> 2) & 15) + 16 * (4 * (pos >> 6) + (pos & 3)) + 256 * kb, km = (2048 - k) & 2047;
        u32 o0 = ((k * 514) & ~3) + 16 * l, om = ((km * 514) & ~3) + (l - 33) * 512;
        PIN(o0); PIN(om);
        om = l < 35 ? om : OOB; wro = l < 33 ? o0 : om;
    }
    u32 tga = OOB;
    if (WARMC) {
        const int Pw = panel_of(L);
        const u32 own = w == 0 ? WS_G0 + kq * 8192 + gcol * 8 : w < 5 ? WS_FL + Pw * 512 + (w - 1) * 128 : (w & 1) ? WS_WB + L * 32 : WS_G1 + L * 32;
        tga = l < 3 ? WS_TK - 8192 + l * 8192 + w * 1024 + 4 * L : l == 3 ? own : OOB;
    }
    tga = l == 0 ? WS_EPOCH : tga;
    PIN(e0a); PIN(wro); PIN(tga); asm volatile("" : "+s"(xso), "+s"(wla));
    SCHED();
    asm volatile("s_waitcnt lgkmcnt(0)" : "+s"(ka), "+s"(kb4));
    unsigned char *const pwm = mkptr(ka[0], ka[1]), *const pc = mkptr(ka[2], ka[3]), *const ws = mkptr(ka[4], ka[5]), *const px = mkptr(ka[6], ka[7]),
                  *const pwr = mkptr(ka[8], ka[9]), *const pwi = mkptr(ka[10], ka[11]), *const pw1 = mkptr(ka[12], ka[13]), *const pw3 = mkptr(ka[14], ka[15]),
                  *const pw2 = mkptr(kb4[0], kb4[1]), *const pout = mkptr(kb4[2], kb4[3]);
    STAMP(0);
    STAMPC(60);
    const rsrc_t rws = mkrs(ws, WS_END);
    const v4u rws4 = mkrs4(ws, WS_END);
    const f4 z4 = {0.f, 0.f, 0.f, 0.f};
    const float fl = (float)li, fg4 = (float)(4 * g), fw = (float)w;
    v4u GR, GI, HR, HI;
    f4 bc, bs, bc2, bs2;
#define TAB(i) lds<v4u>(L_TAB + (i) * 1024 + l * 16)
#define CONSTS_B() do { \
        _Pragma("unroll") for (int r = 0; r < 4; ++r) { \
            const float a3 = (8.f * (fg4 + r) + fw) * fl * (1.f / 2048), a4 = (8.f * fl + fw) * (fg4 + r) * (1.f / 2048); \
            bc[r] = cosr(a3); bs[r] = sinr(a3); bc2[r] = cosr(a4); bs2[r] = sinr(a4); } \
        fwd_pair((8.f * fg4 + fw) * fl * (1.f / 128), fl * (1.f / 16), GR, GI); \
        fwd_pair((8.f * fl + fw) * fg4 * (1.f / 128), (8.f * fl + fw) * (1.f / 128), HR, HI); \
        PIN(GR); PIN(GI); PIN(HR); PIN(HI); PIN(bc); PIN(bs); PIN(bc2); PIN(bs2); } while (0)
    u32 tagv; ld32a(tagv, rws4, tga);
#define LTILE() dma16t(mkrs4(pwm, 512 * 3072 * 2), (l >> 3) * 6144 + ((((l >> 1) & 3) ^ (((l >> 4) & 1) | ((w & 1) << 1))) * 2 + (l & 1)) * 16, \
        ((L >> 5) * 64 + 8 * w) * 6144 + 2048 + (L & 31) * 128, L_LT + w * 1024)
#define ROWS() do { dma16n(mkrs4(px, 2048 * 1024), 16 * l, xso, L_XS + w * 1024); \
        dma16n(mkrs4(pwr, 2048 * 514), wro, 0, wla); dma16n(mkrs4(pwi, 2048 * 514), wro, 0, wla + 1024); } while (0)
    if (TLBW && w == 3) {
        constexpr u32 NWS = WS_END / (TLBW ? TLBW : 1 << 20) + 1, NM = 1792 * 1024 / (TLBW ? TLBW : 1 << 20) + 1;
        static_assert(NWS + 4 * NM <= 64, "lanes");
        const int j = (l - (int)NWS) / (int)NM, r = (l - (int)NWS) % (int)NM;
        const unsigned char* b = l < (int)NWS ? ws : j == 0 ? pw1 : j == 1 ? pw3 : j == 2 ? pw2 : pout;
        const u32 o = l < (int)NWS ? l * TLBW : j < 4 ? r * TLBW : 0;
        asm volatile("s_mov_b32 m0, %1\n\ts_nop 0\n\tglobal_load_lds_dword %0, off" ::"v"(b + o), "s"(L_SCRAP) : "memory");
    }
    v4u e0[4], cv;
    if (w == GW) {
        ld128p(cv, mkrs4(pc, 1024), 16 * l);
        const v4u rwm = mkrs4(pwm, 512 * 3072 * 2);
#pragma unroll
        for (int i = 0; i < 4; ++i) ld128p(e0[i], rwm, e0a, ((i + L) & 3) * (RPI * 6144));
        ROWS();
    }
    SCHED();
    STAMP(33);
    const int P = panel_of(L), mem = ((L >> 5) & 3) * 4 + (L & 3);
    const bool act = (L & IM) != 3;
    const int cbk = 7 * ((L >> 6) & 1) + ((L & 3) < 3 ? 4 * ((L >> 5) & 1) + (L & 3) : 3);
    const int si = 2 * P + ((L >> 6) & 1);
    const v4u rs1 = mkrs4(pw1, act || !(MB | MC) ? 512 * 1792 * 2 : 0), rs3 = mkrs4(pw3, act || !(MB | MC) ? 512 * 1792 * 2 : 0);
    u32 wv1 = (4 * w + g) * 3584 + cbk * 256 + (((li >> 1) ^ g ^ (SH16 ? 4 * (w & 1) : 0)) * 2 + (l & 1)) * 16, wv3 = SH16 ? wv1 : wv1 ^ 128;
    u32 dso[6], dla[6];
#define W13_PREP(first, nb) do { int p_ = (L & 3) < 3 ? (L >> 2) & 7 : (3 * (IM == 7 ? (L >> 3) & 3 : ((L >> 3) & 2) + ((L >> 2) & 1))) >> 1; p_ = p_ >= (nb) ? p_ - (nb) : p_; _Pragma("unroll") for (int i = 0; i < (nb); ++i) { \
        dso[i] = ((first) + p_) * 114688; dla[i] = L_BIG + p_ * 16384 + w * 1024; p_ = p_ + 1 == (nb) ? 0 : p_ + 1; \
        asm volatile("" : "+s"(dso[i]), "+s"(dla[i])); } } while (0)
#define W13_PC(j) dma16d<((j) & 1) * 8192>(((j) & 1) ? rs3 : rs1, ((j) & 1) ? wv3 : wv1, dso[(j) >> 1], dla[(j) >> 1])
#define NX(a) (a)
#define W13_RANGE(a, b) sfor<(b) - (a)>([&](auto i_) { W13_PC((a) + i_.value); })
#define PC(m) __builtin_popcount(m)
#define SPR(k, nb, m) do { if constexpr (((m) >> (k)) & 1) { SCHED(); W13_PC(2 * (nb) - PC((m) >> (k))); SCHED(); } } while (0)
#define SPRG(k, nb, m, d0, d1, st) do { SPR(k, nb, m); gap<(st) - (NX(((m) >> (k)) & 1) ? 3 : 0)>(d0, d1); } while (0)
    u32 trd = (4 * (t & 1)) * 1040 + (t >> 1) * 4;
    u32 rowo = w * 1040 + l * 16, wgo;
    {
        const int kap = t >> 1, k1 = (kap >> 2) & 15, kk = 4 * (kap >> 6) + (kap & 3);
        const int ka_ = kk < 8 ? k1 + 32 * kk : 512 - k1 - 32 * kk, k2 = (k1 == 0 && kk >= 8) ? 32 * kk - 240 : ka_;
        wgo = L_WR + (t & 1) * WRB(4) + (L & 1) * 2 + k2 * 2;
    }
    u32 tk_w = l == 0 ? WS_TK + w * 1024 + 4 * L : OOB, mrd = L_MOD0 + 8 * l, wt_w = x1_w + (WS_WT - WS_X1);
    W13_PREP(0, NB1);
    u32 g0r = t * 16; PIN(g0r);
    PIN(x1_w); PIN(x2_w); PIN(trd); PIN(rowo); PIN(wgo); PIN(tk_w); PIN(mrd); PIN(wt_w); PIN(wv1); PIN(wv3);
    SCHED();
    STAMP(1);
    if (w == GW) {
        u32 g0a = (l & (KS == 2 ? 13 : 12)) == 0 ? WS_G0 + kq * 8192 + (gcol + (l % LPR) * 8 + (l >> 4) * 2 + ((l >> 1) & 1)) * 8 : OOB;
        PIN(g0a);
        waitv<7>(cv); PIN(tagv);
        f4 s0, s1;
#pragma unroll
        for (int i = 0; i < 2; ++i) {
            s0[2 * i] = bfr(silu(lo16(cv[i]))); s0[2 * i + 1] = bfr(silu(hi16(cv[i])));
            s1[2 * i] = bfr(silu(lo16(cv[2 + i]))); s1[2 * i + 1] = bfr(silu(hi16(cv[2 + i])));
        }
        lds<f4>(L_MOD1 + 32 * l) = s0; lds<f4>(L_MOD1 + 32 * l + 16) = s1;
        lds<v4u>(L_MOD1 + 2048 + 16 * l) = v4u{pk(s0[0], s0[1]), pk(s0[2], s0[3]), pk(s1[0], s1[1]), pk(s1[2], s1[3])};
        asm volatile("s_waitcnt lgkmcnt(0)" ::: "memory");
        float sc[4];
#pragma unroll
        for (int i = 0; i < 4; ++i) sc[i] = lds<float>(L_MOD1 + (kq * (512 / KS) + ((i + L) & 3) * RPI + l / LPR) * 4);
        u32 tg = __builtin_amdgcn_readfirstlane(tagv) + 1u;
        asm volatile("" : "+v"(sc[0]), "+v"(sc[1]), "+v"(sc[2]), "+v"(sc[3]), "+v"(tg));
        SCHED();
        STAMP(38);
        f2 a[4] = {{0.f, 0.f}, {0.f, 0.f}, {0.f, 0.f}, {0.f, 0.f}};
        sfor<4>([&](auto i) { waitv<6 - i.value>(e0[i.value]);
#pragma unroll
            for (int j = 0; j < 4; ++j) a[j] += sc[i.value] * f2{lo16(e0[i.value][j]), hi16(e0[i.value][j])};
            SCHED(); });
        const float b0 = tadd16(tadd32(a[0].x, a[2].x), tadd32(a[1].x, a[3].x)), b1 = tadd16(tadd32(a[0].y, a[2].y), tadd32(a[1].y, a[3].y));
        const bool o2 = l & 2;
        float c0 = (o2 ? b1 : b0) + dppget<0x4E>(o2 ? b0 : b1);
        c0 = dppadd<0x124, 0xF>(c0); c0 = dppadd<0x128, 0xF>(c0);
        if (KS == 2) c0 = dppadd<0xB1, 0xF>(c0);
        __builtin_amdgcn_raw_buffer_store_b64(v2u{__builtin_bit_cast(u32, c0), tg}, rws, g0a, 0, SC1);
        STAMP(2);
    } else { ROWS(); SCHED(); if (CBP == 0) CONSTS_B(); }
    SCHED();
    __builtin_amdgcn_s_barrier();
    STAMP(41);
    const bool tw = (TOUCH & 1) ? (w & 6) == 4 : (TOUCH & 2) ? act && w >= 4 && (w < 6 || (L & 3) == 3) : false;
    u32 tch = (TOUCH & 1) ? (l < 56 ? (L * 56 + l) * 128 : OOB) : ((l >> 3) * 32 + (((L >> 2) & 7) ^ (w >> 1 & 1)) * 4 + ((l >> 1) & 3)) * 3584 + cbk * 256 + (l & 1) * 128;
    PIN(tch);
#define WARM2(first, nb) do { if (tw) dma4s((w & 1) ? rs3 : rs1, (TOUCH & 1) || l < 8 * (nb) ? tch : OOB, (first) * 114688, L_SCRAP); } while (0)
#define WARM() WARM2(0, NB1)
    constexpr int NF = F2P == 0 ? 2 : 1;
    dma16(rt, l * 16, w * 1024, L_TAB + w * 1024); dma16(rt, l * 16, w * 1024 + 8192, L_TAB + 8192 + w * 1024);
    if (LPP == 0) LTILE();
    st128c(sent4, rws, x1_w);
    if (F2P == 0) st128c(sent4, rws, x2_w);
    if (TP == 0) WARM();
    if (CBP == 1) CONSTS_B();
    if (w == GW || (TP == 0 && tw)) waitvm<5 + NF + (LPP == 0)>(); else waitvm<4 + NF + (LPP == 0)>();
    f2 xh[4]; v2u xc0, xc1; float mean1;
    {
        const v4u xa = ldtr2(L_XS + w * 1024 + 8 * l, L_XS + w * 1024 + 512 + 8 * l);
        xc0 = lds<v2u>(L_XS + w * 1024 + 64 * li + 8 * g); xc1 = lds<v2u>(L_XS + w * 1024 + 64 * li + 32 + 8 * g);
        f2 v[4];
#pragma unroll
        for (int i = 0; i < 4; ++i) v[i] = f2{lo16(xa[i]), hi16(xa[i])};
        const f2 s = (v[0] + v[1]) + (v[2] + v[3]);
        mean1 = wave_sum(s.x + s.y) * (1.f / 512);
        f2 q = {0.f, 0.f};
#pragma unroll
        for (int i = 0; i < 4; ++i) { v[i] -= mean1; q += v[i] * v[i]; }
        const float rstd = __builtin_amdgcn_rsqf(wave_sum(q.x + q.y) * (1.f / 512) + 1e-5f);
#pragma unroll
        for (int i = 0; i < 4; ++i) { xh[i] = v[i] * rstd; PIN(xh[i]); }
    }
    PIN(tagv);
    const u32 tag = __builtin_amdgcn_readfirstlane(tagv) + 1u;
    SCHED();
    STAMP(39);
    v4u m0[KS];
#define G0_LOAD do { _Pragma("unroll") for (int i = 0; i < KS; ++i) ld128a(m0[i], rws4, g0r, WS_G0 + i * 8192); } while (0)
    if (TP == 0 && tw) waitvm<NF + 1>(); else waitvm<NF>();
    if (G0B) __builtin_amdgcn_s_barrier();
    nap<D_E0>();
    G0_LOAD;
    BAR();
    STAMP(40);
    if (LPP == 1) LTILE();
    if (TP == 1) WARM();
    v4u wo4;
#pragma unroll
    for (int j = 0; j < 4; ++j) wo4[j] = (u32)lds<u16>(wgo + j * WRS) | ((u32)lds<u16>(wgo + j * WRS + 1024) << 16);
    const v4u aBR = TAB(T_aBR), aBI = TAB(T_aBI), FR = TAB(T_FR), aAI = TAB(T_aAI), aSR = TAB(T_aSR), aSI = TAB(T_aSI);
    const f4 tc = __builtin_bit_cast(f4, (v4u)TAB(T_tc)), ts = __builtin_bit_cast(f4, (v4u)TAB(T_ts));
    SCHED();
    if (w == 0) {
        const u32 b = L_WR + WRB(l & 7) + (L & 1) * 2;
#define WE(off) lo16((u32)lds<u16>(b + (off)))
        const float h0r = WE(0) + WE(528), h0i = WE(1024) - WE(1024 + 528), h1r = WE(512) + WE(544), h1i = WE(1024 + 512) - WE(1024 + 544);
        const float sg = (l & 8) ? -0.25f : 0.25f;
        const int ab = (int)pk(0.25f * h0r + sg * h1r, 0.25f * h0i + sg * h1i);
        v4u q;
#pragma unroll
        for (int j = 0; j < 4; ++j) q[j] = (u32)__builtin_amdgcn_ds_bpermute(((l & 3) * 4 + j) * 4, ab);
        if (l < 2) wo4 = q;
        st128c(q, rws, (l & 62) == 2 ? WS_WB + L * 32 + (l & 1) * 16 : OOB);
    }
#define HOP1() do { if ((w & 6) == 2) {     \
        const u32 bo = L_LT + (8 * g + ((l >> 2) & 3)) * 128 + ((2 * (w & 1)) ^ ((l >> 3) & 3)) * 32 + (l & 3) * 8, ao = L_MOD1 + 2048 + (L >> 5) * 128 + g * 16; \
        const v4u s0 = lds<v4u>(ao), s1 = lds<v4u>(ao + 64); \
        const f4 d0 = mfma16(s1, ldtr2(bo + 4096, bo + 4608), mfma16(s0, ldtr2(bo, bo + 512), z4)); \
        const f4 d1 = mfma16(s1, ldtr2((bo ^ 32) + 4096, (bo ^ 32) + 4608), mfma16(s0, ldtr2(bo ^ 32, (bo ^ 32) + 512), z4)); \
        __builtin_amdgcn_raw_buffer_store_b64(v2u{__builtin_bit_cast(u32, (float)(g ? d1[0] : d0[0])), tag}, rws, l < 32 ? WS_LP + L * 512 + (w & 1) * 256 + l * 8 : OOB, 0, SC1); } } while (0)
    if (LPP == 0) HOP1();
    st128c(wo4, rws, wt_w);
    if ((LPP == 0 ? w < 4 && w != 1 : w == 0) || (TP < 2 && tw)) waitvm<KS + NF + 1 + (LPP == 1)>(); else waitvm<KS + NF + (LPP == 1)>();
    __builtin_amdgcn_raw_buffer_store_b32(tag, rws, tk_w, 0, SC1);
    tk_w += WS_TKB - WS_TK; PIN(tk_w);
    if (F2P == 1) st128c(sent4, rws, x2_w);
    if (LPP == 2) LTILE();
    SCHED();
    STAMP(3);
    W13_RANGE(0, N1);
    if (TP == 2) WARM();
    SCHED();
    STAMP(32);
    {
        if ((LPP == 0 ? w < 4 && w != 1 : w == 0) || ((TP == 1 || TP == 2) && tw)) waitvm<3 + F2P + NX(N1) + (LPP == 1 || LPP == 2)>(); else waitvm<2 + F2P + NX(N1) + (LPP == 1 || LPP == 2)>();
#pragma unroll
        for (int i = 0; i < KS; ++i) PIN(m0[i]);
#define G0_BAD (KS == 2 ? ((m0[0].y ^ tag) | (m0[0].w ^ tag) | (m0[1].y ^ tag) | (m0[1].w ^ tag)) \
                        : ((m0[0].y ^ tag) | (m0[0].w ^ tag) | (m0[1].y ^ tag) | (m0[1].w ^ tag) | (m0[KS - 2].y ^ tag) | (m0[KS - 2].w ^ tag) | (m0[KS - 1].y ^ tag) | (m0[KS - 1].w ^ tag)))
        RETRY(G0_BAD != 0, { G0_LOAD; waitv<0>(m0[0]); _Pragma("unroll") for (int i = 1; i < KS; ++i) PIN(m0[i]); });
#define ASF(x) __builtin_bit_cast(float, (u32)(x))
        float v0 = ASF(m0[0].x) + ASF(m0[1].x), v1 = ASF(m0[0].z) + ASF(m0[1].z);
        if (KS == 4) { v0 += ASF(m0[KS - 2].x) + ASF(m0[KS - 1].x); v1 += ASF(m0[KS - 2].z) + ASF(m0[KS - 1].z); }
        u32 p = pk(v0, v1);
        if (w >= 4) p = pk(lo16(p) + 1.f, hi16(p) + 1.f);
        lds<u32>(L_MOD0 + t * 4) = p;
    }
    BAR();
    STAMP(4);
    if (LPP == 3) LTILE();
    if (TP == 3) WARM();

    {
        const v4u sa = ldtr2(mrd, mrd + 512), ca = ldtr2(mrd + 1024, mrd + 1536);
        SPR(0, NB1, 0); SPR(1, NB1, 0);
        f4 yr, yi;
        mf2<false>(yr, yi, sa, aBR, sa, aBI);
        v4u ha;
#pragma unroll
        for (int i = 0; i < 4; ++i) { const f2 h = xh[i] * f2{lo16(ca[i]), hi16(ca[i])}; ha[i] = pk(h.x, h.y); }
        mf2acc(yr, yi, ha, aBR, aBI);
        SPRG(2, NB1, 0, yr, yi, 8);
        const v4u yb = twid_pack(yr, yi, tc, ts);
        f4 xr, xi, sr, si4;
        mf2<true>(xr, xi, FR, yb, aAI, yb); mf2<false>(sr, si4, aSR, yb, aSI, yb);
        SPRG(3, NB1, 0, xr, xi, 6);
        v4u o1 = pk4(xr, xi); PIN(o1);
        asm volatile("" : "+v"(sr), "+v"(si4));
        v4u o2 = pk4(sr, si4); PIN(o2);
        v4u o = li ? o1 : o2;
        lds<v4u>(L_T1 + rowo) = o;
        SCHED();
        if (!LTK) {
        if ((TP == 2 || TP == 3) && tw) waitvm<1 + NX(N1 + NA) + (LPP == 3)>(); else waitvm<NX(N1 + NA) + (LPP == 3)>();
        __builtin_amdgcn_raw_buffer_store_b32(tag, rws, tk_w, 0, SC1);
        }
        BAR();
#pragma unroll
        for (int j = 0; j < 4; ++j) o[j] = lds<u32>(L_T1 + trd + j * 1040);
        st128c(o, rws, x1_w);
    }
    SCHED();
    STAMP(5);
    if (LPP > 0 && LPP < 3 && !LTK) HOP1();
    u32 tk, tkb, tk_r = WS_TK + (L >> 5) * 1024 + (t >> 1) * 4;
    const u32 h2_w = (P * 128 + mem * 8 + w) * 1024 + (4 * li + g) * 16;
    u32 h2_a = h2_w + WS_H2;
    u32 xrd = (t >> 1) * XSTR + kapB * 32 + (t & 1) * 16, x1_r = WS_X1 + xrd, wt_r = WS_WT + xrd, wb_r = WS_WB + t * 16;
    PIN(tk_r); PIN(h2_a); PIN(x1_r); PIN(wt_r); PIN(wb_r);
    __builtin_amdgcn_s_barrier();
    if (L == 0 && w == 7) __builtin_amdgcn_raw_buffer_store_b32(tag, rws, l == 0 ? WS_EPOCH : OOB, 0, SC1);
    v2u pp;
    u32 lp_r = WS_LP + (l >> 3) * 16384 + L * 64 + (l & 7) * 8;
    if (LPP == 0 && w == 3) ld64a(pp, rws4, lp_r);
    ld32a(tk, rws4, tk_r);
    if (TP == 4) { __builtin_amdgcn_s_barrier(); WARM(); }
    constexpr int NW1 = 2 * NB1 - PC(MB);
    if (LTK) {
        static_assert(!LTK || (N1 == 0 && TP == 3 && LPP == 1), "");
        if (act) { W13_RANGE(0, NK); if (tw) waitvm<3 + NX(NK)>(); else waitvm<2 + NX(NK)>(); } else waitvm<2>();
        __builtin_amdgcn_raw_buffer_store_b32(tag, rws, tk_w, 0, SC1);
        __builtin_amdgcn_s_barrier();
        asm volatile("" ::: "memory");
        HOP1();
    }
    if (act) W13_RANGE(LTK ? NK : N1, NW1); else nap<D_I1>();
    if (!LTK) ld32a(tkb, rws4, tk_r + (WS_TKB - WS_TK));
    if (!FLH) st128c(sent4, rws, h2_a);
    else if (w < 2) st128c(sent4, rws, WS_H2 + (P * 128 + 2 * mem + w) * 1024 + l * 16);
    u32 e1w = w * 1088 + l * 16, eo = 2 * g * 1088 + (32 * w + 2 * li) * 4;
    u32 pm0 = 0, pm1 = 0;
    if (kapB == 0) {
#pragma unroll
        for (int jb = 0; jb < 2; ++jb) {
            const int pos = 32 * w + 2 * li + jb, q = ((pos >> 2) & 15) + 16 * (4 * (pos >> 6) + (pos & 3)), qm = (256 - q) & 255;
            (jb ? pm1 : pm0) = L_E1 + 2 * g * 1088 + (64 * (qm >> 6) + 4 * (qm & 15) + ((qm >> 4) & 3)) * 4;
        }
    }
    PIN(e1w); PIN(eo); PIN(pm0); PIN(pm1);
    const bool tw3 = (TOUCH & 6) && act && w < 4 && (w < 2 || (L & 3) == 3);
    tch = l < 48 ? ((l >> 3) * 32 + (((L >> 2) & 7) ^ (w >> 1 & 1)) * 4 + ((l >> 1) & 3)) * 3584 + cbk * 256 + (l & 1) * 128 : OOB; PIN(tch);
#define WARM3(first, nb) do { if (tw3) dma4s((w & 1) ? rs3 : rs1, tch, (first) * 114688, L_SCRAP); } while (0)
    if (CBP == 2 || (CBP == 0 && w == GW)) CONSTS_B();
    const v4u FI = TAB(T_FI), A81 = TAB(T_A8);
    const v2u A8 = {A81.x, A81.y}, A1 = {A81.z, A81.w};
    if (act) waitvm<NX(2 * NB1 - PC(MB) - N1) + 2 - FLH>(); else if (FLH && w >= 2) waitvm<1>(); else waitvm<2>();
    PIN(tk);
    RETRY(tk != tag, { ld32a(tk, rws4, tk_r); waitv<0>(tk); });
#define HOP2() do { if (w == 3) { \
        PIN(pp); \
        RETRY(pp.y != tag, { ld64a(pp, rws4, lp_r); waitv<0>(pp); }); \
        float a = __builtin_bit_cast(float, (u32)pp.x); \
        a = dppadd<0x128, 0xF>(a); a = swapadd16(a); a = swapadd32(a); \
        a = bfr(a) + ((L >> 6) == 2 ? 1.f : 0.f); \
        __builtin_amdgcn_raw_buffer_store_b64(v2u{pk(a, dppget<0xB1>(a)), tag}, rws, (l & 57) == 0 ? WS_G1 + L * 32 + l * 4 : OOB, 0, SC1); \
    } } while (0)
    if (LPP == 0) HOP2();
    if (WX & 32) __builtin_amdgcn_s_barrier();
    if (WX & 1) waitvm<1>();
    STAMP(6);
    nap<D_E1>();
    v4u zin, wf4, wb4;
    if (LTK) ld32a(tkb, rws4, tk_r + (WS_TKB - WS_TK));
    ld128a(zin, rws4, x1_r);
    __builtin_amdgcn_s_barrier();
    static_assert(LPP > 0 || !FLH, "");
    if (LPP == 0 && w == 3) waitvm<3>(); else if (LTK || (FLH && w >= 2)) waitvm<1>(); else waitvm<2>();
    PIN(tkb);
    if (LTK) RETRY(tkb != tag, { ld32a(tkb, rws4, tk_r + (WS_TKB - WS_TK)); waitv<0>(tkb); });
    else if (__builtin_expect(anylane(tkb != tag), 0)) { waitv<0>(zin); RETRY(vmax(zin, 0) == SENT, { ld128a(zin, rws4, x1_r); waitv<0>(zin); }); }
    ld128a(wf4, rws4, wt_r);
    if (LPP == 3) HOP1(); else if (LPP > 0 && w == 3) ld64a(pp, rws4, lp_r);
    if (!act) { ld128a(wb4, rws4, kapB == 0 ? wb_r : OOB); waitvm<2>(); } else if (LPP == 3 ? (w & 6) == 2 : LPP > 0 && w == 3) waitvm<2>(); else waitvm<1>();
    PIN(zin);
    STAMP(7);

    {
        f4 re, im;
        mf2<false>(re, im, zin, FR, zin, FI);
        PIN(zin);
        if (__builtin_expect(anylane(vmax(zin, 0) == SENT), 0)) {
            RETRY(vmax(zin, 0) == SENT, { ld128a(zin, rws4, x1_r); waitv<0>(zin); });
            mf2<false>(re, im, zin, FR, zin, FI); gap<8>(re, im);
        }
        if (TOUCH & 6) WARM3(NB1, NB2);
        SPRG(0, NB1, MB, re, im, 5);
        v4u zb = twid_pack(re, im, bc, bs);
        mf2<true>(re, im, GR, zb, GI, zb);
        SPRG(1, NB1, MB, re, im, 8);
        lds<v4u>(L_E1 + e1w) = pk4(re, im);
        SPR(2, NB1, MB);
        BAR();
        const v2u u0 = lds<v2u>(L_E1 + eo), u1 = lds<v2u>(L_E1 + eo + 1088);
        SPR(3, NB1, MB);
        f4 d0, d1;
        mf2k(d0, d1, A8, v2u{u0.x, u1.x}, v2u{u0.y, u1.y});
        if (tw3) waitvm<NX(PC(MB & 15)) + 1>(); else waitvm<NX(PC(MB & 15))>();
        PIN(wf4);
        f2 wq[4];
#pragma unroll
        for (int j = 0; j < 4; ++j) wq[j] = f2{lo16(wf4[j]), hi16(wf4[j])};
        asm volatile("" : "+v"(wq[0]), "+v"(wq[1]), "+v"(wq[2]), "+v"(wq[3]), "+v"(d0), "+v"(d1));
        f2 f[4] = {cmul(f2{d0[0], d0[1]}, wq[0]), cmul(f2{d0[2], d0[3]}, wq[1]), cmul(f2{d1[0], d1[1]}, wq[2]), cmul(f2{d1[2], d1[3]}, wq[3])};
        if (kapB == 0) {
            PIN(wb4);
            const v2u um0 = {lds<u32>(pm0), lds<u32>(pm0 + 1088)}, um1 = {lds<u32>(pm1), lds<u32>(pm1 + 1088)};
            f4 mz = mfma16k(A8 ^ 0x80000000u, um0, z4), m0_, m1_;
            mf2k(m0_, m1_, A1, um0, um1); gap<8>(m0_, m1_);
            if ((w | li) == 0) m0_ = mz;
            f[0] += cmul(f2{m0_[0], m0_[1]}, f2{lo16(wb4[0]), hi16(wb4[0])}); f[1] += cmul(f2{m0_[2], m0_[3]}, f2{lo16(wb4[1]), hi16(wb4[1])});
            f[2] += cmul(f2{m1_[0], m1_[1]}, f2{lo16(wb4[2]), hi16(wb4[2])}); f[3] += cmul(f2{m1_[2], m1_[3]}, f2{lo16(wb4[3]), hi16(wb4[3])});
        }
        mf2k(d0, d1, A8, v2u{pk(f[0].y, f[0].x), pk(f[1].y, f[1].x)}, v2u{pk(f[2].y, f[2].x), pk(f[3].y, f[3].x)});
        SPRG(4, NB1, MB, d0, d1, 8);
        lds<v2u>(L_E2 + eo) = v2u{pk(d0[0], d0[1]), pk(d1[0], d1[1])};
        lds<v2u>(L_E2 + eo + 1088) = v2u{pk(d0[2], d0[3]), pk(d1[2], d1[3])};
        SPR(5, NB1, MB);
        BAR();
        zb = lds<v4u>(L_E2 + e1w);
        if (LPP == 3 && w == 3) ld64a(pp, rws4, lp_r);
        SPR(6, NB1, MB);
        mf2<false>(re, im, zb, HR, zb, HI);
        SPRG(7, NB1, MB, re, im, 8);
        zb = twid_pack(re, im, bc2, bs2);
        mf2<true>(re, im, FR, zb, FI, zb);
        gap<8>(re, im);
        st128c(pk4(im, re), rws, x2_w);
    }
    SCHED();
    STAMP(8);
    if (LPP == 3 && w == 3) waitvm<1>();
    if (LPP > 0) HOP2();
    v4u wf[32];
    u32 wtr = L_BIG + (g & 1) * 8192 + (4 * (l >> 5) + ((l >> 2) & 3)) * 256 + ((w ^ ((l >> 2) & 3) ^ ((g & 1) * 4)) * 32) + (((l & 1) << 1) | ((l >> 1) & 1)) * 8;
    if (SH16) wtr = L_BIG + (l >> 2) * 256 + ((w ^ ((l >> 2) & 7)) * 32) + (l & 3) * 8;
    u32 wtr2 = wtr + 65536;
    PIN(wtr); PIN(wtr2);
#define W13_FRAG(first, j) wf[2 * (first) + (j)] = ldtr2((((j) >> 3) ? wtr2 : wtr) + (((j) >> 1) & 3) * 16384 + ((j) & 1) * (SH16 ? 8192 : 2048), (((j) >> 3) ? wtr2 : wtr) + (((j) >> 1) & 3) * 16384 + ((j) & 1) * (SH16 ? 8192 : 2048) + 4096)
#define W13_READ(first, nb) do { _Pragma("unroll") for (int j = 0; j < 2 * (nb); ++j) W13_FRAG(first, j); } while (0)
    if (MB) { waitvm<1>(); __builtin_amdgcn_s_barrier(); asm volatile("" ::: "memory"); }
    if (act) W13_READ(0, NB1);
    u32 x2_r = WS_X2 + (xrd ^ 96), g1_r = WS_G1 + t * 16;
    W13_PREP(NB1, NB2);
    PIN(x2_r); PIN(g1_r);
    BAR();
    v4u m1;
    if (LPP == 0) ld128a(m1, rws4, g1_r);
    if (act) W13_RANGE(0, 2 * NB2 - PC(MC)); else nap<D_I2>();
    if (WX & 16) waitvm<NX(2 * NB2 - PC(MC))>();
    if (WX & 8) __builtin_amdgcn_s_barrier();
    if (WX & 2) waitvm<0>();
    STAMP(9);
    nap<D_E2>();
    if (LPP > 0) ld128a(m1, rws4, g1_r);
    ld128a(zin, rws4, x2_r);
    const v4u cBR = TAB(T_cBR), cBI = TAB(T_cBI), cSR = TAB(T_cSR), cSI = TAB(T_cSI), cB0 = TAB(T_cB0), cB1 = TAB(T_cB1);
    u32 m1w = L_MF + (t >> 7) * 2048 + ((t >> 2) & 1) * 1024 + (t & 3) * 256 + ((t >> 3) & 15) * 16;
#define UNP(v) f2{lo16(v), hi16(v)}
    f2 xp[4] = {UNP(xc0.x) - mean1, UNP(xc0.y) - mean1, UNP(xc1.x) - mean1, UNP(xc1.y) - mean1};
    PIN(m1w); PIN(xp[0]); PIN(xp[1]); PIN(xp[2]); PIN(xp[3]);
    waitv<1>(m1);
    RETRY(((m1.y ^ tag) | (m1.w ^ tag)) != 0, { ld128a(m1, rws4, g1_r); waitv<0>(m1); });
    lds<f4>(m1w) = f4{lo16(m1.x), hi16(m1.x), lo16(m1.z), hi16(m1.z)};
    waitv<0>(zin);
    STAMP(10);

    v4u xo; u32 xr_w;
    {
#define ZPUT do { _Pragma("unroll") for (int j = 0; j < 4; ++j) lds<u32>(L_T0 + trd + j * 1040) = zin[j]; } while (0)
        ZPUT;
        SCHED();
        if (__builtin_expect(anylane(vmax(zin, 0) == SENT), 0)) { RETRY(vmax(zin, 0) == SENT, { ld128a(zin, rws4, x2_r); waitv<0>(zin); }); ZPUT; }
        if (TOUCH & 6) WARM3(NB1 + NB2, NB3);
        SPR(0, NB2, MC);
        BAR();
        const v4u va = lds<v4u>(L_T0 + rowo);
        SCHED();
        const f4 ga0 = lds<f4>(L_MF + l * 16), ga1 = lds<f4>(L_MF + 1024 + l * 16), cb0 = lds<f4>(L_MF + 4096 + l * 16), cb1 = lds<f4>(L_MF + 5120 + l * 16),
                 sb0 = lds<f4>(L_MF + 2048 + l * 16), sb1 = lds<f4>(L_MF + 3072 + l * 16);
        SCHED();
        SPR(1, NB2, MC); SPR(2, NB2, MC);
        f4 sr, si4, ur, ui;
        mf2<false>(sr, si4, va, cSR, va, cSI); mf2<false>(ur, ui, va, cBR, va, cBI);
        SPRG(3, NB2, MC, ur, ui, 8);
        asm volatile("" : "+v"(sr), "+v"(si4));
        if (g == 0) { ur[0] = sr[0]; ui[0] = si4[0]; }
        f4 tr = ur * tc - ui * ts, ti = ui * tc + ur * ts;
        PIN(tr); PIN(ti);
        const v4u ta = pk4(tr, ti);
        f4 y0, y1;
        mf2<true>(y0, y1, ta, cB0, ta, cB1);
        SPRG(4, NB2, MC, y0, y1, 8);
        const f4 v0 = f4{xp[0].x, xp[0].y, xp[1].x, xp[1].y} + ga0 * y0, v1 = f4{xp[2].x, xp[2].y, xp[3].x, xp[3].y} + ga1 * y1;
        const f4 p1 = v0 + v1, p2 = v0 * v0 + v1 * v1;
        const f2 s1 = f2{p1[0], p1[1]} + f2{p1[2], p1[3]}, s2 = f2{p2[0], p2[1]} + f2{p2[2], p2[3]};
        SPR(5, NB2, MC);
        const f2 ss = wave_sum2(s1.x + s1.y, s2.x + s2.y) * (1.f / 512);
        const float m2 = ss.x, rstd = __builtin_amdgcn_rsqf(fmaxf(ss.y - m2 * m2, 0.f) + 1e-5f);
        const f4 h0 = ((v0 - m2) * cb0) * rstd + sb0, h1 = ((v1 - m2) * cb1) * rstd + sb1;
        st128c(v4u{pk(h0[0], h0[1]), pk(h0[2], h0[3]), pk(h1[0], h1[1]), pk(h1[2], h1[3])}, rws, h2_a);
        SCHED();
        STAMP(11);
        if (MC) { waitvm<1>(); __builtin_amdgcn_s_barrier(); asm volatile("" ::: "memory"); }
        if (act) W13_READ(NB1, NB2);
        W13_PREP(NB1 + NB2, NB3);
        const f4 x0 = v0 + mean1, x1 = v1 + mean1;
        const auto r0 = __builtin_amdgcn_permlane16_swap(pk(x0[0], x0[1]), pk(x1[0], x1[1]), false, false);
        const auto r1 = __builtin_amdgcn_permlane16_swap(pk(x0[2], x0[3]), pk(x1[2], x1[3]), false, false);
        xo = v4u{r0[0], r1[0], r0[1], r1[1]};
        xr_w = WS_XR + (P * 128 + mem * 8 + w) * 1024 + li * 64 + (g & 1) * 32 + (g >> 1) * 16;
    }

    PIN(xo); PIN(xr_w);
    BAR();
    if (act) W13_RANGE(0, 2 * NB3);

    const v4u rs2 = mkrs4(pw2, 1792 * 512 * 2);
    const int P2 = L >> 4, rp = (L >> 3) & 1, ct = L & 7;
#define W2_DECL u32 v2a = (l >> 3) * 1024 + ct * 128 + ((((l >> 1) & 3) ^ ((l >> 4) & 1)) * 2 + (l & 1)) * 16, v2b = v2a ^ 64; \
        v4u w2f[28], hb[2][7]; u32 w2a[4], fl16
#define W2_ADDR() do { const int l_ = fresh_lane(); _Pragma("unroll") for (int nt = 0; nt < 4; ++nt) { \
        w2a[nt] = L_PARK + w * 12288 + (8 * (l_ >> 4) + ((l_ >> 2) & 3)) * 128 + (l_ & 3) * 8 + ((nt ^ ((l_ >> 3) & 3)) * 32); PIN(w2a[nt]); } } while (0)
#define W2_FRAG(i) if (ABL & 16) asm volatile("" : "=v"(w2f[i])); else w2f[i] = ldtr2(w2a[(i) & 3] + (((i) >> 2) % 3) * 4096, w2a[(i) & 3] + (((i) >> 2) % 3) * 4096 + 512)
    u32 kof[7];
    {
        int r = ROT ? (ct + 2 * ((L >> 3) & 3)) % 7 : 0;
#pragma unroll
        for (int k = 0; k < 7; ++k) { kof[k] = (7 * w + r) * 1024; r = r == 6 ? 0 : r + 1; asm volatile("" : "+s"(kof[k])); }
    }
    const u32 w2l = L_PARK + w * 12288;
#define W2_DMA(i) dma16c<((i) & 3) * 8192, ((i) % 12) * 1024, false>(rs2, ((i) & 1) ? v2b : v2a, kof[(i) >> 2] * 32, w2l)
#define PW(w) ((w) & 3)
#define FL_ADDR(f) (WS_FL + ((f) == 3 ? P : P2) * 512 + (f) * 128 + (fresh_lane() & 15) * 4)
#define FL_LOAD(f) fl16 = __builtin_amdgcn_raw_buffer_load_b32(rws, FL_ADDR(f), 0, SC1)
#define FL_STORE(f) do { if (w == 0) __builtin_amdgcn_raw_buffer_store_b32(tag, rws, fresh_lane() == 0 ? WS_FL + P * 512 + (f) * 128 + mem * 4 : 0x7FFFFFF0u, 0, SC1); } while (0)
#define H_LOAD1(q, k) hb[(q) & 1][k] = ld128c(rws, h_rd + (q) * 114688, kof[k])
#define H_LOAD(q) do { _Pragma("unroll") for (int k = 0; k < 7; ++k) H_LOAD1(q, k); } while (0)
#define G2_STEP(q, k) do { _Pragma("unroll") for (int nt = 0; nt < 4; ++nt) { \
        if ((k) == 0) mfma16z(o[nt], w2f[nt], hb[(q) & 1][0]); else mfma16a(o[nt], w2f[4 * (k) + nt], hb[(q) & 1][k]); } } while (0)

    auto gemm2 = [&](const v4u (&w2f)[28], v4u (&hb)[2][7], u32 h_rd, bool slow) __attribute__((always_inline)) {
        STAMP(13); STAMPC(62);
        const int l = fresh_lane(), li = l & 15, g = l >> 4;
        const int qq = w >> 1, cg = l & 7, rs_ = 8 * (w & 1) + (l >> 3), pr = (2 * qq + rp) * 16 + rs_;
        v4u xa, g0, g1;
        u32 pw0, pw1, po, po2, oo, fl16, mx = 0; f4 x0, x1, b0, b1, oa[2][4];
#define P_WRITE(q, nt) lds<f4>(((nt) & 1 ? pw1 : pw0) + ((q) < 3 ? (q) * 4096 : 0) + (nt) * 1024) = oa[(q) & 1][nt]
        sfor<28>([&](auto ss) {
            constexpr int st = ss.value, q = st / 7, k = st % 7;
            if (st == 0) prio<2>(); if (st == 10) prio<1>(); if (st == 19) prio<0>();
            { auto& o = oa[q & 1]; G2_STEP(q, k); }
            SCHED();
            if (st == 0) {
                pw0 = L_PARK + w * 12288 + g * 256 + (li ^ ((g >> 1) << 2)) * 16; pw1 = pw0 ^ 128; PIN(pw0); PIN(pw1);
            }
            if (q > 0 && k >= 1 && k < 5) { PIN(oa[(q - 1) & 1][k - 1]); P_WRITE(q - 1, k - 1); }
            if (q == 3) { mx = vmax(hb[1][k], mx); PIN(mx); }
            if (st == FLK) FL_LOAD(2);
            if (st >= 3 && st < 7) H_LOAD1(1, k);
            if (st == 8) { RETRY(fl16 != tag, { if (slow) nap<8>(); FL_LOAD(2); }); H_LOAD1(2, 0); }
            if (st >= 8 && st < 21) H_LOAD1(q + 1, k);
            if (st == 21) xa = ld128c(rws, (l >> 3) * 1024 + cg * 16, WS_XR + (P2 * 128 + (2 * qq + rp) * 16 + 8 * (w & 1)) * 1024 + ct * 128);
            if (st == 21) { g0 = ld128c(rws, cg * 32, WS_G1 + 6144 + ct * 256); g1 = ld128c(rws, cg * 32 + 16, WS_G1 + 6144 + ct * 256); }
            if (st == 1) { po = (qq < 3 ? L_PARK + qq * 4096 : 0) + (cg >> 1) * 1024 + (cg & 1) * 512 + (rs_ ^ ((cg & 3) << 2)) * 16; po2 = po + (qq < 3 ? 49152 : 4096); PIN(po); PIN(po2); }
            if (st == 2) {
                const int m2 = pr >> 3, L2 = (P2 >> 3) * 128 + (m2 >> 2) * 32 + ((P2 & 7) ^ (((m2 >> 2) & 1) * (IM >> 2))) * 4 + (m2 & 3);
                oo = row_of(L2, pr & 7) * 1024 + ct * 128 + cg * 16; PIN(oo);
            }
            if (st == 26) {
                const u32 d = L_PARK + (w >> 2) * (49152 - 4096); pw0 -= d; pw1 -= d; PIN(pw0); PIN(pw1);
            }
            if (k == 6) STAMP(34 + q);
            SCHED();
        });
        x0 = f4{lo16(xa.x), hi16(xa.x), lo16(xa.y), hi16(xa.y)}; x1 = f4{lo16(xa.z), hi16(xa.z), lo16(xa.w), hi16(xa.w)};
        asm volatile("" : "+v"(x0), "+v"(x1), "+v"(oa[1][0]), "+v"(oa[1][1]), "+v"(oa[1][2]), "+v"(oa[1][3]), "+v"(mx));
        if (__builtin_expect(anylane(mx == SENT), 0)) {
            u32 sp_ = 0;
#pragma nounroll
            do { if (slow) nap<4>(); asm volatile("" ::: "memory"); H_LOAD(3); } while (anylane(vmaxn<7>(hb[1]) == SENT) && ++sp_ < SPIN_CAP);
            auto& o = oa[1];
#pragma unroll
            for (int k = 0; k < 7; ++k) { asm volatile("s_nop 1" ::: "memory"); G2_STEP(3, k); }
            asm volatile("s_nop 7" : "+v"(o[0]), "+v"(o[1]), "+v"(o[2]), "+v"(o[3]));
        }
        P_WRITE(3, 0); P_WRITE(3, 1); P_WRITE(3, 2); P_WRITE(3, 3);
        SCHED();
        b0 = f4{lo16(g0.x), hi16(g0.x), lo16(g0.z), hi16(g0.z)}; b1 = f4{lo16(g1.x), hi16(g1.x), lo16(g1.z), hi16(g1.z)};
        PIN(b0); PIN(b1);
        BAR();
        STAMP(14);
        f4 pa[8], pb[8];
#pragma unroll
        for (int i = 0; i < 8; ++i) { const u32 o_ = (i < 4 ? po : po2) + (i & 3) * 12288; pa[i] = lds<f4>(o_); pb[i] = lds<f4>(o_ + 256); }
        SCHED();
        f4 s0 = pa[0] + pa[1], s1 = pb[0] + pb[1];
#pragma unroll
        for (int i = 2; i < 8; ++i) { s0 += pa[i]; s1 += pb[i]; }
        x0 += b0 * s0; x1 += b1 * s1;
        __builtin_amdgcn_raw_buffer_store_b128(v4u{pk(x0[0], x0[1]), pk(x0[2], x0[3]), pk(x1[0], x1[1]), pk(x1[2], x1[3])}, mkrs(pout, 2048 * 1024), oo, 0, 0);
        STAMP(15); STAMPC(63);
        STAMP_FLUSH();
    };

    if (!act) {
        if (W2W && w < 7) dma4(rs2, ((si >> 2) * 224 + w * 32 + (si & 3) * 8) * 1024 + l * 128, L_SCRAP);
        st128c(xo, rws, xr_w);
    }
    int L_ = L; asm volatile("" : "+s"(L_));
    if (act) {
        const int l = fresh_lane();
        u32 a_v = (l >> 3) * 1024 + (((l & 7) ^ (4 * (w & 1) + (l >> 4))) * 16);
        u32 a_s = WS_H2 + (P * 128 + 8 * (w & 3)) * 1024 + (w >> 2) * 128, a_l = w * 1024;
#define A_DMA(n, h) dma16c<((n) >> 1) * 32768 + ((n) & 1) * 512 + (h) * 256, ((n) & 3) * SLOT + (h) * 8192, true>(rws4, a_v, a_s, a_l)
#define A_RD(n) do { ck0 = lds<v4u>(a_c + ((n) & 3) * SLOT); ck1 = lds<v4u>(a_c + ((n) & 3) * SLOT + 8192); } while (0)
#define A_CHK(n) RETRY(vmax(ck1, vmax(ck0, 0)) == SENT, { A_DMA(n, 0); A_DMA(n, 1); VM0(); A_RD(n); })
        W2_DECL;
        const int ft = 8 * cbk + w;
        u32 hh_w = WS_HH + (P * 8 + ((l >> 4) & 1)) * 57344 + (ft >> 1) * 1024 + ((ft & 1) * 2 + (l >> 5)) * 256 + (l & 15) * 16;
        u32 a_c = w * 1024 + l * 16;
        u32 b_r[4];
#pragma unroll
        for (int i = 0; i < 4; ++i) { b_r[i] = SH16 ? (l & 15) * 128 + (((2 * (i & 2) + (l >> 4)) ^ ((l >> 1) & 7)) * 16) : (l & 31) * 128 + (((2 * i + (l >> 5)) ^ ((l >> 1) & 7)) * 16); if (!SH16 || !(i & 1)) PIN(b_r[i]); }
        u32 h_rd = 0;
        v4u ck0, ck1;
        PIN(a_v); PIN(a_c); PIN(v2a); PIN(v2b); PIN(hh_w); PIN(xr_w); PIN(xo);
        asm volatile("" : "+s"(a_s), "+s"(a_l));
        SCHED();
        if (WX & 4) waitvm<0>();
        STAMP(43);
        nap<D_E3>();
        A_DMA(0, 0); A_DMA(0, 1);
        waitvm<2>();
        __builtin_amdgcn_s_barrier();
        if (FLH) FL_STORE(3);
        asm volatile("" ::: "memory");
        W13_READ(NB1 + NB2, NB3);
        SCHED();
        if (FLH && w == 0) waitvm<1>(); else waitvm<0>();
        A_RD(0); A_CHK(0);
        A_DMA(1, 0); A_DMA(1, 1);
        BAR();
        STAMP(12); STAMPC(61);
        v4u bf[2 * NBP];
        f4 acc[2][4];
#define B_ONE(J, hf) bf[2 * ((J) % NBP) + (hf)] = lds<v4u>(b_r[2 * ((J) & 1)] + (((J) >> 3) & 3) * SLOT + (((J) & 7) >> 1) * 4096 + (hf) * 2048)
#define HH_STORE(mt) do { PIN(hh_w); const auto s0_ = __builtin_amdgcn_permlane16_swap(hp[0], hp[2], false, false); \
        const auto s1_ = __builtin_amdgcn_permlane16_swap(hp[1], hp[3], false, false); \
        st128c(v4u{s0_[0], s1_[0], s0_[1], s1_[1]}, rws, hh_w, (mt) * 114688); } while (0)
#define GATE(mt, r) acc[(mt) & 1][((r) >> 2) * 2][(r) & 3]
#define UPV(mt, r) acc[(mt) & 1][((r) >> 2) * 2 + 1][(r) & 3]
#define ACC_NOP(n, i) asm volatile("s_nop " #n : "+v"(acc[i][0]), "+v"(acc[i][1]), "+v"(acc[i][2]), "+v"(acc[i][3]))
#define G1_MFMA() do { if (kh == 0 && u < 2) { mfma16z(acc[mt & 1][2 * (u & 1)], wf[0], bf[2 * (J % NBP) + (u & 1)]); mfma16z(acc[mt & 1][2 * (u & 1) + 1], wf[1], bf[2 * (J % NBP) + (u & 1)]); } \
        else { mfma16a(acc[mt & 1][2 * (u & 1)], wf[16 * kh + (u & 14)], bf[2 * (J % NBP) + (u & 1)]); mfma16a(acc[mt & 1][2 * (u & 1) + 1], wf[16 * kh + (u & 14) + 1], bf[2 * (J % NBP) + (u & 1)]); } } while (0)
#define B_READ(J) do { B_ONE(J, 1); SCHED(); B_ONE(J, 0); } while (0)
#define EP_A(mt, r) do { hv[(r) & 1] = __builtin_amdgcn_exp2f(-1.44269504089f * GATE(mt, r)); pv = GATE(mt, r) * UPV(mt, r); PIN(hv[(r) & 1]); PIN(pv); } while (0)
#define EP_B(mt, r) do { hv[(r) & 1] = pv * __builtin_amdgcn_rcpf(1.0f + hv[(r) & 1]); \
        if ((r) & 1) { hp[(r) >> 1] = pk(hv[0], hv[1]); PIN(hp[(r) >> 1]); } else PIN(hv[0]); } while (0)
#define G2(mt, p) f2{GATE(mt, 2 * (p)), GATE(mt, 2 * (p) + 1)}
#define EP_Q(mt, p, s_) do { \
        if ((s_) == 0) { ev = G2(mt, p) * -1.44269504089f; PIN(ev); ev.x = __builtin_amdgcn_exp2f(ev.x); PIN(ev); } \
        if ((s_) == 1) { ev.y = __builtin_amdgcn_exp2f(ev.y); gu = G2(mt, p) * f2{UPV(mt, 2 * (p)), UPV(mt, 2 * (p) + 1)}; PIN(gu); PIN(ev); } \
        if ((s_) == 2) { ev += 1.0f; PIN(ev); ev.x = __builtin_amdgcn_rcpf(ev.x); PIN(ev); } \
        if ((s_) == 3) { ev.y = __builtin_amdgcn_rcpf(ev.y); PIN(ev); const f2 h_ = gu * ev; hp[p] = pk(h_.x, h_.y); PIN(hp[p]); } } while (0)
        B_READ(0); B_READ(1); if (NBP == 3) B_READ(2);
#define FL3_BAD(q) ((__builtin_amdgcn_ballot_w64(fl16 != tag) >> (4 * (q))) & 15)
#define FL3_CHK(q) do { if (__builtin_expect(FL3_BAD(q) != 0, 0)) { u32 sp_ = 0; \
        _Pragma("nounroll") do { ld32a(fl16, rws4, FL_ADDR(3)); waitv<0>(fl16); } while (FL3_BAD(q) != 0 && ++sp_ < SPIN_CAP); } } while (0)
#define G1WAIT(op) do { static_assert(g1pos(op) < T, "program order"); waitvm<g1after(op, T)>(); } while (0)
#define W2_FREE(i, Tread) static_assert(g1pos(OP_D + (i)) >= (Tread) + 5, "slot still being read")
        W2_FREE(12, 48 + 5); W2_FREE(16, 48 + 9); W2_FREE(20, 48 + 13); W2_FREE(24, 96 + 1);
        float hv[2], pv; f2 ev, gu; u32 hp[4] = {1, 2, 3, 4};
        sfor<8>([&](auto nn) {
            constexpr int n = nn.value, mt = n >> 1, kh = n & 1;
            sfor<16>([&](auto uu) {
                constexpr int u = uu.value, T = 16 * n + u, J = T >> 1, op = g1op(T);
                constexpr bool wide = op == OP_XR || op == OP_FILL || (op >= OP_HH && op < OP_FL);
                auto store = [&]() __attribute__((always_inline)) {
                    if (op == OP_XR) st128c(xo, rws, xr_w);
                    else if (op == OP_FILL) { PIN(hh_w); st128c(sent4, rws, hh_w, 3 * 114688); }
                    else HH_STORE(op - OP_HH);
                };
                if (!FEWBAR || n < 2) { if (u == 0) prio<2>(); if (u == 4) prio<1>(); if (u == 8) prio<0>(); }
                else { if ((T & 31) == 6) prio<2>(); if ((T & 31) == 14) prio<1>(); if ((T & 31) == 22) prio<0>(); }
                G1_MFMA();
                SCHED();
                if constexpr (u == 13 && (!FEWBAR || n == 0 || (n & 1))) {
                    if constexpr (n == 3 || n == 5 || n == 7) G1WAIT(OP_HH + (n - 3) / 2);
                    BAR();
                    prio<3>();
                    STAMP(17 + 2 * n);
                }
                {
                    constexpr bool bar = !FEWBAR || n == 0 || (n & 1);
                    if (NBP == 3 && bar && u == 13 && J + 2 < 64) B_READ(J + 2);
                    if ((u & 1) && J + NBP < 64 && !(NBP == 3 && bar && u == 11)) B_READ(J + NBP);
                }
                if (u == 13 && (n == 3 || n == 5 || n == 7)) FL_STORE((n - 3) >> 1);
                if constexpr (!FEWBAR || n == 0) {
                    if constexpr (u == (n ? 5 : 9) && n < 7) { G1WAIT(OP_A + 2 * n + 3); if (!(ABL & 4)) A_RD(n + 1); }
                    if (!(ABL & 4) && u == (n ? 9 : 11) && n < 7) A_CHK(n + 1);
                    if constexpr (FLH && T == 7) { G1WAIT(OP_FL + 3); PIN(fl16); FL3_CHK(1); }
                } else if constexpr (FLH) {
                    if constexpr ((n & 1) && n < 7 && u == 12) G1WAIT(OP_A + 2 * n + 5);
                    if constexpr (T + 1 == g1pos(OP_A + 8)) FL3_CHK(2);
                    if constexpr (T + 1 == g1pos(OP_A + 12)) FL3_CHK(3);
                } else if constexpr ((n & 1) && n < 7) {
                    if constexpr (u == 3) { G1WAIT(OP_A + 2 * n + 3); if (!(ABL & 4)) A_RD(n + 1); }
                    if constexpr (u == 7) { if (!(ABL & 4)) A_CHK(n + 1); G1WAIT(OP_A + 2 * n + 5); if (!(ABL & 4)) A_RD(n + 2); }
                    if (!(ABL & 4) && u == 11) A_CHK(n + 2);
                }
                if constexpr (T == 48 + 1) { W2_ADDR(); G1WAIT(OP_D + 7); }
                if constexpr (T == 48 + 9) G1WAIT(OP_D + 11);
                if constexpr (n == 3 && u >= 2 && u < 14) W2_FRAG(u - 2);
                if constexpr (T >= 80 + 14 && T < 96 + 2) W2_FRAG(T - 82);
                if constexpr (T == 112) { G1WAIT(OP_FL); PIN(fl16); RETRY(fl16 != tag, FL_LOAD(0)); }
                if (T == 112 + 1) { h_rd = WS_HH + (P2 * 8 + rp) * 57344 + fresh_lane() * 16; PIN(h_rd); }
                if constexpr (n == 7 && u >= 1 && u < 9) W2_FRAG(15 + u);
                if constexpr (T == 112 + 9) G1WAIT(OP_D + 27);
                if constexpr (n == 7 && u >= 9 && u < 13) W2_FRAG(15 + u);
                if constexpr (!(ABL & 1) && op >= OP_D && op < OP_A) W2_DMA(op);
                if constexpr ((!(ABL & 2) || n == 0) && op >= OP_A && op < OP_XR) A_DMA((op - OP_A) >> 1, op & 1);
                if constexpr (op >= OP_FL && op < OP_H) ld32a(fl16, rws4, FL_ADDR(op - OP_FL));
                if constexpr (op >= OP_H) { if (ABL & 64) asm volatile("" : "=v"(hb[0][op - OP_H])); else H_LOAD1(0, op - OP_H); }
                if (!(ABL & 32) && n >= 2) {
                    if ((T & 31) == EP0) { if (SH16) ACC_NOP(0, (mt - 1) & 1); else ACC_NOP(3, (mt - 1) & 1); }
                    sfor<16>([&](auto hh2) { constexpr int h = hh2.value;
                        if ((h * (EPN + 1 - EP0)) / 16 + EP0 == (T & 31)) { if (EPK) EP_Q(mt - 1, h >> 2, h & 3); else if (h & 1) EP_B(mt - 1, h >> 1); else EP_A(mt - 1, h >> 1); } });
                }
                if (!(ABL & 8) && wide) store();
                SCHED();
            });
        });
        STAMP(42); STAMPC(59);
        waitvm<g1after(OP_FL + 1, 128)>();
        PIN(fl16);
        H_LOAD1(1, 0); H_LOAD1(1, 1); H_LOAD1(1, 2);
        SCHED();
        {
            ACC_NOP(7, 1);
#pragma unroll
            for (int r = 0; r < 4; ++r) hp[r] = pk(silu(GATE(3, 2 * r)) * UPV(3, 2 * r), silu(GATE(3, 2 * r + 1)) * UPV(3, 2 * r + 1));
            HH_STORE(3);
        }
        SCHED();
        if (__builtin_expect(anylane(fl16 != tag), 0)) {
            RETRY(fl16 != tag, FL_LOAD(1));
#pragma unroll
            for (int k = 0; k < 3; ++k) reload128(hb[1][k], rws4, h_rd + 114688, kof[k]);
        }
        gemm2(w2f, hb, h_rd, false);
    }
    if ((L_ & IM) == 3) {
        const u32 c0 = (u32)__builtin_amdgcn_s_memtime();
        const int l = fresh_lane();
        W2_DECL;
        W2_ADDR();
        if (FLH) { if (w < 7) waitvm<1 + W2W>(); else waitvm<1>(); __builtin_amdgcn_s_barrier(); FL_STORE(3); }
        nap<D_IW>();
        sfor<3>([&](auto r) {
            sfor<12>([&](auto i) { if constexpr (12 * r.value + i.value < 28) W2_DMA(12 * r.value + i.value); });
            VM0();
            if (r.value == 0) { BAR(); FL_STORE(0); FL_STORE(1); FL_STORE(2); }
            sfor<12>([&](auto i) { if constexpr (12 * r.value + i.value < 28) W2_FRAG(12 * r.value + i.value); });
            asm volatile("s_waitcnt lgkmcnt(0)" ::: "memory");
        });
        while ((u32)__builtin_amdgcn_s_memtime() - c0 < C_IDLE) nap<4>();
        FL_LOAD(1);
        RETRY(fl16 != tag, { nap<12>(); FL_LOAD(1); });
        const u32 h_rd = WS_HH + (P2 * 8 + rp) * 57344 + l * 16;
        H_LOAD(0); H_LOAD1(1, 0); H_LOAD1(1, 1); H_LOAD1(1, 2);
        gemm2(w2f, hb, h_rd, true);
        asm volatile(".Lcode_tail:");
    }
}

extern "C" void kernel_launch(void* const* d_in, const int* in_sizes, int n_in, void* d_out, int out_size, void* d_ws, size_t ws_size,
                              hipStream_t stream) {
    (void)in_sizes; (void)n_in; (void)out_size; (void)ws_size;
    const Args a = {(const u16*)d_in[8], (const u16*)d_in[7], (unsigned char*)d_ws, (const u16*)d_in[0], (const u16*)d_in[1],
                    (const u16*)d_in[2], (const u16*)d_in[4], (const u16*)d_in[5], (const u16*)d_in[6], (u16*)d_out};
    fused_kernel<<<256, 512, 0, stream>>>(a);
}
```
